```python
import torch

def _softmax(x, axis=-1):
    return torch.softmax(x, dim=axis)
def _gelu(x):
    return torch.nn.functional.gelu(x, approximate="tanh")
def _silu(x):
    return torch.nn.functional.silu(x)
def _sigmoid(x):
    return torch.sigmoid(x)
def _layernorm(x, axis=-1, eps=1e-5):
    m = x.mean(dim=axis, keepdim=True)
    v = x.var(dim=axis, keepdim=True, unbiased=False)
    return (x - m) / torch.sqrt(v + eps)
def _scatter_add(tgt, idx, src, axis=0):
    return tgt.clone().index_add_(axis, idx, src)
def _segment_sum(src, seg, n):
    out = torch.zeros((n,) + src.shape[1:], dtype=src.dtype)
    return out.index_add_(0, seg, src)
def _pad(x, pw):
    flat = tuple(v for pair in reversed(pw) for v in pair)
    return torch.nn.functional.pad(x, flat)

kernel_name = 'gated_mix__linear_attn_block__dense_activation_relu__prompt'

def setup_inputs():

    _w = (lambda a: (lambda t: t.to(torch.bfloat16) if t.is_floating_point() else t)(torch.as_tensor(a)))
    def _A_setup():
        B, S, D = 1, 2048, 576
        return {
            "x": _w((lambda *s: torch.randn(*s, dtype=torch.bfloat16) * 0.05)(B, S, D)),
            "wqkv": _w((lambda *s: torch.randn(*s, dtype=torch.bfloat16) * 0.8)(D, 3 * D)),
            "wo": _w((lambda *s: torch.randn(*s, dtype=torch.bfloat16) * 0.2)(D, D)),
        }
    _A_in = _A_setup()

    def _B_setup():
        B, S, D, Dff = 1, 2048, 576, 4608
        return {
            "x": _w((lambda *s: torch.randn(*s, dtype=torch.bfloat16) * 0.05)(B, S, D)),
            "w1": _w((lambda *s: torch.randn(*s, dtype=torch.bfloat16) * 0.05)(D, Dff)),
            "w2": _w((lambda *s: torch.randn(*s, dtype=torch.bfloat16) * 0.05)(Dff, D)),
        }
    _B_in = _B_setup()

    return {
        "A_x": _A_in["x"],
        "A_wqkv": _A_in["wqkv"],
        "A_wo": _A_in["wo"],
        "B_x": _B_in["x"],
        "B_w1": _B_in["w1"],
        "B_w2": _B_in["w2"],
        "gate_w": _w((lambda *s: torch.randn(*s, dtype=torch.bfloat16) * 0.05)(576) * 16.0),
    }

def reference(A_x, A_wqkv, A_wo, B_x, B_w1, B_w2, gate_w):
    def _A_ref(x, wqkv, wo):
        B, S, D = x.shape
        H = 32; Dh = D // H
        qkv = (lambda x, *d: x.permute(*d))((x @ wqkv).reshape(B, S, 3, H, Dh), 2, 0, 3, 1, 4)
        q, k, v = qkv[0], qkv[1], qkv[2]
        phi = lambda t: torch.where(t > 0, t + 1, torch.exp(t)) + 1e-6
        pq, pk = phi(q), phi(k)
        kv = torch.einsum("bhsd,bhse->bhde", pk, v)
        z = torch.einsum("bhsd->bhd", pk)
        num = torch.einsum("bhqd,bhde->bhqe", pq, kv)
        den = torch.einsum("bhqd,bhd->bhq", pq, z)[..., None] + 1e-6
        o = num / den
        return (lambda x, *d: x.permute(*d))(o, 0, 2, 1, 3).reshape(B, S, D) @ wo

    def _B_ref(x, w1, w2):
        h = torch.einsum("bsd,df->bsf", x, w1)
        zero = torch.zeros_like(h)
        h = torch.maximum(h, zero)
        return torch.einsum("bsf,fd->bsd", h, w2)

    x = A_x
    g = _sigmoid((x * gate_w).sum(-1, keepdims=True))
    a = _A_ref(x, A_wqkv, A_wo)
    b = _B_ref(x, B_w1, B_w2)
    return g * a + (1 - g) * b
```

```cpp
#include <hip/hip_runtime.h>
#include <hip/hip_bf16.h>
#include <stdint.h>

#define S_ 2048
#define D_ 576
#define NQKV 1728
#define DFF 4608
#define NH 32
#define DH 18

typedef __attribute__((ext_vector_type(8))) short bf16x8;
typedef __attribute__((ext_vector_type(4))) short bf16x4;
typedef __attribute__((ext_vector_type(4))) float f32x4;
typedef __attribute__((address_space(3))) bf16x4* lds_b4_ptr;
typedef __attribute__((address_space(3))) void* lds_vptr;

__device__ __forceinline__ unsigned short f2bf(float f) {
  unsigned u = __float_as_uint(f);
  u += 0x7fffu + ((u >> 16) & 1u);
  return (unsigned short)(u >> 16);
}
__device__ __forceinline__ float bf2f(unsigned short h) { return __uint_as_float(((unsigned)h) << 16); }

#define WAIT_VM0 asm volatile("s_waitcnt vmcnt(0)" ::: "memory")
#define WAIT_LGKM0 asm volatile("s_waitcnt lgkmcnt(0)" ::: "memory")

template <int BM, int BN, int WAVES_M, int WAVES_N>
struct Gemm {
  static constexpr int WMT = BM / WAVES_M, WNT = BN / WAVES_N;
  static constexpr int MT = WMT / 16, NT = WNT / 16;
  static constexpr int A_BYTES = BM * 128, B_ROWB = BN * 2, B_BYTES = 64 * B_ROWB;
  static constexpr int STAGE_BYTES = A_BYTES + B_BYTES;
  static constexpr int A_INS = A_BYTES / 1024 / 8, B_INS = B_BYTES / 1024 / 8;
  static constexpr int UPR = BN / 16;

  static __device__ __forceinline__ int bswz(int k) {
    if (UPR >= 8) return (k & 3) | (((k >> 3) & 1) << 2);
    else return ((k >> 1) & 1) | (((k >> 3) & 1) << 1);
  }

  static __device__ __forceinline__ void stage(char* buf, const unsigned short* A, long lda, int m0, int ka,
                                               const unsigned short* B, long ldb, int kb, int n0, int ncols,
                                               int wave, int lane) {
#pragma unroll
    for (int i = 0; i < A_INS; ++i) {
      int off = (wave * A_INS + i) * 1024 + lane * 16;
      int row = off >> 7, slot = (off & 127) >> 4;
      int ch = slot ^ ((row >> 1) & 7);
      const unsigned short* g = A + (long)(m0 + row) * lda + ka + ch * 8;
      __builtin_amdgcn_global_load_lds((const unsigned*)g, (lds_vptr)(buf + off), 16, 0, 0);
    }
#pragma unroll
    for (int i = 0; i < B_INS; ++i) {
      int off = (wave * B_INS + i) * 1024 + lane * 16;
      int krow = off / B_ROWB, slot = (off % B_ROWB) >> 4;
      int ch = slot ^ (bswz(krow) << 1);
      int col = n0 + ch * 8;
      if (col > ncols - 8) col = ncols - 8;
      const unsigned short* g = B + (long)(kb + krow) * ldb + col;
      __builtin_amdgcn_global_load_lds((const unsigned*)g, (lds_vptr)(buf + A_BYTES + off), 16, 0, 0);
    }
  }

  static __device__ __forceinline__ void compute(const char* buf, f32x4 (&acc)[MT][NT], int wm, int wn, int lane) {
    const int fr = lane & 15, g = lane >> 4, q = (lane & 15) >> 2, p = lane & 3;
#pragma unroll
    for (int s = 0; s < 2; ++s) {
      bf16x8 af[MT], bfr[NT];
#pragma unroll
      for (int i = 0; i < MT; ++i) {
        int row = wm * WMT + 16 * i + fr;
        int ch = 4 * s + g;
        af[i] = *(const bf16x8*)(buf + row * 128 + 16 * (ch ^ ((row >> 1) & 7)));
      }
#pragma unroll
      for (int c = 0; c < NT; ++c) {
        int u = (wn * WNT + 16 * c) >> 4;
        int k0 = 32 * s + 8 * g + q;
        int k1 = k0 + 4;
        bf16x4 lo = __builtin_amdgcn_ds_read_tr16_b64_v4i16(
            (lds_b4_ptr)(buf + A_BYTES + k0 * B_ROWB + 32 * (u ^ bswz(k0)) + 8 * p));
        bf16x4 hi = __builtin_amdgcn_ds_read_tr16_b64_v4i16(
            (lds_b4_ptr)(buf + A_BYTES + k1 * B_ROWB + 32 * (u ^ bswz(k1)) + 8 * p));
        bfr[c] = __builtin_shufflevector(lo, hi, 0, 1, 2, 3, 4, 5, 6, 7);
      }
#pragma unroll
      for (int i = 0; i < MT; ++i)
#pragma unroll
        for (int c = 0; c < NT; ++c)
          acc[i][c] = __builtin_amdgcn_mfma_f32_16x16x32_bf16(bfr[c], af[i], acc[i][c], 0, 0, 0);
    }
  }
};

typedef Gemm<256, 256, 2, 4> G1;

__global__ __launch_bounds__(512, 2) void k1_gemm(const unsigned short* __restrict__ x,
                                                  const unsigned short* __restrict__ wqkv,
                                                  const unsigned short* __restrict__ w1,
                                                  unsigned short* __restrict__ qkv,
                                                  unsigned short* __restrict__ h) {
  extern __shared__ __attribute__((aligned(16))) char smem[];
  const int tid = threadIdx.x, wave = tid >> 6, lane = tid & 63;
  const int wm = wave >> 2, wn = wave & 3;
  int bid = blockIdx.x;
  const unsigned short* B;
  int ldb, n0, m0;
  bool is_h;
  if (bid < 144) { is_h = true; B = w1; ldb = DFF; n0 = (bid % 18) * 256; m0 = (bid / 18) * 256; }
  else { bid -= 144; is_h = false; B = wqkv; ldb = NQKV; n0 = (bid % 7) * 256; m0 = (bid / 7) * 256; }

  f32x4 acc[G1::MT][G1::NT];
#pragma unroll
  for (int i = 0; i < G1::MT; ++i)
#pragma unroll
    for (int c = 0; c < G1::NT; ++c) acc[i][c] = (f32x4){0.f, 0.f, 0.f, 0.f};

  const int nt = D_ / 64;
  G1::stage(smem, x, D_, m0, 0, B, ldb, 0, n0, ldb, wave, lane);
  for (int t = 0; t < nt; ++t) {
    WAIT_VM0;
    __builtin_amdgcn_s_barrier();
    if (t + 1 < nt)
      G1::stage(smem + ((t + 1) & 1) * G1::STAGE_BYTES, x, D_, m0, (t + 1) * 64, B, ldb, (t + 1) * 64, n0, ldb, wave, lane);
    G1::compute(smem + (t & 1) * G1::STAGE_BYTES, acc, wm, wn, lane);
  }
  __builtin_amdgcn_s_barrier();
  char* wbuf = smem + wave * 16384;
  const int fr = lane & 15, g = lane >> 4;
#pragma unroll
  for (int i = 0; i < G1::MT; ++i)
#pragma unroll
    for (int c = 0; c < G1::NT; ++c) {
      int row = 16 * i + fr, col = 16 * c + 4 * g;
      int gcol = n0 + wn * 64 + col;
      bf16x4 v;
#pragma unroll
      for (int r = 0; r < 4; ++r) {
        float t = acc[i][c][r];
        if (is_h) t = fmaxf(t, 0.f);
        else if (gcol < 2 * D_) t = (t > 0.f ? t + 1.f : __expf(t)) + 1e-6f;
        v[r] = (short)f2bf(t);
      }
      *(bf16x4*)(wbuf + row * 128 + (((col >> 3) ^ (row & 7)) << 4) + ((col & 4) << 1)) = v;
    }
  WAIT_LGKM0;
  unsigned short* dst = is_h ? h : qkv;
#pragma unroll
  for (int it = 0; it < 16; ++it) {
    int row = it * 8 + (lane >> 3), ch = lane & 7;
    bf16x8 v = *(const bf16x8*)(wbuf + row * 128 + ((ch ^ (row & 7)) << 4));
    int gcol = n0 + wn * 64 + ch * 8;
    if (gcol < ldb) *(bf16x8*)(dst + (long)(m0 + wm * 128 + row) * ldb + gcol) = v;
  }
}

__global__ __launch_bounds__(384) void k2a_kv(const unsigned short* __restrict__ qkv, float* __restrict__ kvpart) {
  __shared__ float pk[256][DH + 1];
  __shared__ float vv[256][DH + 1];
  const int head = blockIdx.x & 31, chunk = blockIdx.x >> 5;
  const int tid = threadIdx.x;
  for (int i = tid; i < 256 * DH; i += 384) {
    int r = i / DH, d = i % DH;
    const unsigned short* row = qkv + (long)(chunk * 256 + r) * NQKV;
    pk[r][d] = bf2f(row[D_ + head * DH + d]);
    vv[r][d] = bf2f(row[2 * D_ + head * DH + d]);
  }
  __syncthreads();
  if (tid < DH * DH + DH) {
    float s = 0.f;
    if (tid < DH * DH) {
      int d = tid / DH, e = tid % DH;
      for (int r = 0; r < 256; ++r) s += pk[r][d] * vv[r][e];
    } else {
      int d = tid - DH * DH;
      for (int r = 0; r < 256; ++r) s += pk[r][d];
    }
    kvpart[((long)chunk * NH + head) * (DH * DH + DH) + tid] = s;
  }
}

__global__ __launch_bounds__(256) void k2b_o(const unsigned short* __restrict__ qkv, const float* __restrict__ kvpart,
                                             const unsigned short* __restrict__ x, const unsigned short* __restrict__ gate_w,
                                             unsigned short* __restrict__ o, float* __restrict__ gbuf) {
  __shared__ float kv[4][DH * DH + DH];
  __shared__ float gs[128];
  const int rb = blockIdx.x >> 3, hg = blockIdx.x & 7;
  const int tid = threadIdx.x;
  for (int i = tid; i < 4 * (DH * DH + DH); i += 256) {
    int hh = i / (DH * DH + DH), j = i % (DH * DH + DH);
    float s = 0.f;
    for (int c = 0; c < 8; ++c) s += kvpart[((long)c * NH + hg * 4 + hh) * (DH * DH + DH) + j];
    kv[hh][j] = s;
  }
  {
    int r = tid >> 1, half = tid & 1;
    const unsigned short* xr = x + (long)(rb * 128 + r) * D_ + half * 288;
    const unsigned short* gw = gate_w + half * 288;
    float s = 0.f;
    for (int d = 0; d < 288; ++d) s += bf2f(xr[d]) * bf2f(gw[d]);
    s += __shfl_xor(s, 1);
    float gg = 1.f / (1.f + __expf(-s));
    if (half == 0) {
      gs[r] = gg;
      if (hg == 0) gbuf[rb * 128 + r] = gg;
    }
  }
  __syncthreads();
  for (int pr = tid; pr < 512; pr += 256) {
    int r = pr >> 2, hh = pr & 3;
    const unsigned short* qr = qkv + (long)(rb * 128 + r) * NQKV + (hg * 4 + hh) * DH;
    float pq[DH];
#pragma unroll
    for (int d = 0; d < DH; ++d) pq[d] = bf2f(qr[d]);
    float den = 1e-6f;
#pragma unroll
    for (int d = 0; d < DH; ++d) den += pq[d] * kv[hh][DH * DH + d];
    float sc = gs[r] / den;
    unsigned short* orow = o + (long)(rb * 128 + r) * D_ + (hg * 4 + hh) * DH;
#pragma unroll
    for (int e = 0; e < DH; ++e) {
      float num = 0.f;
#pragma unroll
      for (int d = 0; d < DH; ++d) num += pq[d] * kv[hh][d * DH + e];
      orow[e] = f2bf(num * sc);
    }
  }
}

typedef Gemm<256, 64, 4, 2> G3;

__global__ __launch_bounds__(512, 2) void k3_gemm(const unsigned short* __restrict__ o, const unsigned short* __restrict__ h,
                                                  const unsigned short* __restrict__ wo, const unsigned short* __restrict__ w2,
                                                  const float* __restrict__ gbuf, float* __restrict__ slabs) {
  extern __shared__ __attribute__((aligned(16))) char smem[];
  const int tid = threadIdx.x, wave = tid >> 6, lane = tid & 63;
  const int wm = wave >> 1, wn = wave & 1;
  const int bid = blockIdx.x;
  const int split = bid % 3, tile = bid / 3;
  const int n0 = (tile % 9) * 64, m0 = (tile / 9) * 256;
  const int fr = lane & 15, g = lane >> 4;

  f32x4 acc[G3::MT][G3::NT];
#pragma unroll
  for (int i = 0; i < G3::MT; ++i)
#pragma unroll
    for (int c = 0; c < G3::NT; ++c) acc[i][c] = (f32x4){0.f, 0.f, 0.f, 0.f};

  const int nb = (split == 0) ? 18 : 27;
  const int b0 = (split == 0) ? 0 : 18 + (split - 1) * 27;
  const int nt = (split == 0) ? 27 : 27;

  auto do_stage = [&](int t, char* buf) {
    if (t < nb) {
      int kt = b0 + t;
      G3::stage(buf, h, DFF, m0, kt * 64, w2, D_, kt * 64, n0, D_, wave, lane);
    } else {
      int kt = t - nb;
      G3::stage(buf, o, D_, m0, kt * 64, wo, D_, kt * 64, n0, D_, wave, lane);
    }
  };
  auto scale_acc = [&]() {
#pragma unroll
    for (int i = 0; i < G3::MT; ++i) {
      float sc = 1.f - gbuf[m0 + wm * G3::WMT + 16 * i + fr];
#pragma unroll
      for (int c = 0; c < G3::NT; ++c) acc[i][c] *= sc;
    }
  };

  do_stage(0, smem);
  for (int t = 0; t < nt; ++t) {
    WAIT_VM0;
    __builtin_amdgcn_s_barrier();
    if (t + 1 < nt) do_stage(t + 1, smem + ((t + 1) & 1) * G3::STAGE_BYTES);
    G3::compute(smem + (t & 1) * G3::STAGE_BYTES, acc, wm, wn, lane);
    if (t == nb - 1) scale_acc();
  }
  float* dst = slabs + (long)split * S_ * D_;
#pragma unroll
  for (int i = 0; i < G3::MT; ++i)
#pragma unroll
    for (int c = 0; c < G3::NT; ++c) {
      int row = m0 + wm * G3::WMT + 16 * i + fr, col = n0 + wn * G3::WNT + 16 * c + 4 * g;
      *(f32x4*)(dst + (long)row * D_ + col) = acc[i][c];
    }
}

__global__ __launch_bounds__(256) void k4_reduce(const float* __restrict__ slabs, unsigned short* __restrict__ out) {
  int i = blockIdx.x * 256 + threadIdx.x;
  const f32x4* s0 = (const f32x4*)slabs;
  const f32x4* s1 = (const f32x4*)(slabs + (long)S_ * D_);
  const f32x4* s2 = (const f32x4*)(slabs + 2L * S_ * D_);
  f32x4 v = s0[i] + s1[i] + s2[i];
  bf16x4 r;
#pragma unroll
  for (int j = 0; j < 4; ++j) r[j] = (short)f2bf(v[j]);
  ((bf16x4*)out)[i] = r;
}

extern "C" void kernel_launch(void* const* d_in, const int* in_sizes, int n_in, void* d_out, int out_size, void* d_ws,
                              size_t ws_size, hipStream_t stream) {
  const unsigned short* x = (const unsigned short*)d_in[0];
  const unsigned short* wqkv = (const unsigned short*)d_in[1];
  const unsigned short* wo = (const unsigned short*)d_in[2];
  const unsigned short* w1 = (const unsigned short*)d_in[4];
  const unsigned short* w2 = (const unsigned short*)d_in[5];
  const unsigned short* gate_w = (const unsigned short*)d_in[6];
  unsigned short* out = (unsigned short*)d_out;

  char* ws = (char*)d_ws;
  unsigned short* qkv = (unsigned short*)ws;
  unsigned short* h = (unsigned short*)(ws + 8u * 1024 * 1024);
  unsigned short* o = (unsigned short*)(ws + 28u * 1024 * 1024);
  float* gbuf = (float*)(ws + 31u * 1024 * 1024);
  float* kvpart = (float*)(ws + 31u * 1024 * 1024 + 65536);
  float* slabs = (float*)(ws + 32u * 1024 * 1024);

  hipFuncSetAttribute((const void*)k1_gemm, hipFuncAttributeMaxDynamicSharedMemorySize, 2 * G1::STAGE_BYTES);
  hipFuncSetAttribute((const void*)k3_gemm, hipFuncAttributeMaxDynamicSharedMemorySize, 2 * G3::STAGE_BYTES);

  k1_gemm<<<144 + 56, 512, 2 * G1::STAGE_BYTES, stream>>>(x, wqkv, w1, qkv, h);
  k2a_kv<<<256, 384, 0, stream>>>(qkv, kvpart);
  k2b_o<<<128, 256, 0, stream>>>(qkv, kvpart, x, gate_w, o, gbuf);
  k3_gemm<<<72 * 3, 512, 2 * G3::STAGE_BYTES, stream>>>(o, h, wo, w2, gbuf, slabs);
  k4_reduce<<<S_ * D_ / 4 / 256, 256, 0, stream>>>(slabs, out);
}
```

```cpp
#include <hip/hip_runtime.h>
#include <hip/hip_bf16.h>
#include <stdint.h>

#define S_ 2048
#define D_ 576
#define NQKV 1728
#define DFF 4608
#define NH 32
#define DH 18

typedef __attribute__((ext_vector_type(8))) short bf16x8;
typedef __attribute__((ext_vector_type(4))) short bf16x4;
typedef __attribute__((ext_vector_type(4))) float f32x4;
typedef __attribute__((address_space(3))) bf16x4* lds_b4_ptr;
typedef __attribute__((address_space(3))) void* lds_vptr;

__device__ __forceinline__ unsigned short f2bf(float f) {
  unsigned u = __float_as_uint(f);
  u += 0x7fffu + ((u >> 16) & 1u);
  return (unsigned short)(u >> 16);
}
__device__ __forceinline__ float bf2f(unsigned short h) { return __uint_as_float(((unsigned)h) << 16); }

__device__ __forceinline__ void glds16(unsigned voff, const void* sbase, unsigned lds_dst) {
  unsigned keep;
  asm volatile("s_mov_b32 %0, m0\n\ts_mov_b32 m0, %3\n\ts_nop 0\n\tglobal_load_lds_dwordx4 %1, %2\n\ts_mov_b32 m0, %0"
               : "=&s"(keep) : "v"(voff), "s"(sbase), "s"(lds_dst) : "memory");
}
#define WAIT_VM(n) asm volatile("s_waitcnt vmcnt(" #n ")" ::: "memory")
#define WAIT_LGKM0 asm volatile("s_waitcnt lgkmcnt(0)" ::: "memory")

template <int BM, int BN, int WAVES_M, int WAVES_N>
struct Gemm {
  static constexpr int WMT = BM / WAVES_M, WNT = BN / WAVES_N;
  static constexpr int MT = WMT / 16, NT = WNT / 16;
  static constexpr int A_BYTES = BM * 128, B_ROWB = BN * 2, B_BYTES = 64 * B_ROWB;
  static constexpr int STAGE_BYTES = A_BYTES + B_BYTES;
  static constexpr int A_INS = A_BYTES / 1024 / 8, B_INS = B_BYTES / 1024 / 8;
  static constexpr int UPR = BN / 16;

  static __device__ __forceinline__ int bswz(int k) {
    if (UPR >= 8) return (k & 3) | (((k >> 3) & 1) << 2);
    else return ((k >> 1) & 1) | (((k >> 3) & 1) << 1);
  }
  static __device__ __forceinline__ void offsets(unsigned (&aoff)[A_INS], unsigned (&boff)[B_INS], int lda, int ldb,
                                                 int ncols_left, int wave, int lane) {
#pragma unroll
    for (int i = 0; i < A_INS; ++i) {
      int off = (wave * A_INS + i) * 1024 + lane * 16;
      int row = off >> 7, slot = (off & 127) >> 4;
      int ch = slot ^ ((row >> 1) & 7);
      aoff[i] = (unsigned)(row * lda + ch * 8) * 2u;
    }
#pragma unroll
    for (int i = 0; i < B_INS; ++i) {
      int off = (wave * B_INS + i) * 1024 + lane * 16;
      int krow = off / B_ROWB, slot = (off % B_ROWB) >> 4;
      int ch = slot ^ (bswz(krow) << 1);
      int col = ch * 8;
      if (col > ncols_left - 8) col = ncols_left - 8;
      boff[i] = (unsigned)(krow * ldb + col) * 2u;
    }
  }
  static __device__ __forceinline__ void stage(unsigned lds, const unsigned short* Abase, const unsigned short* Bbase,
                                               const unsigned (&aoff)[A_INS], const unsigned (&boff)[B_INS], int wave) {
#pragma unroll
    for (int i = 0; i < A_INS; ++i) glds16(aoff[i], Abase, lds + (wave * A_INS + i) * 1024);
#pragma unroll
    for (int i = 0; i < B_INS; ++i) glds16(boff[i], Bbase, lds + A_BYTES + (wave * B_INS + i) * 1024);
  }

  static __device__ __forceinline__ void compute(const char* buf, f32x4 (&acc)[MT][NT], int wm, int wn, int lane) {
    const int fr = lane & 15, g = lane >> 4, q = (lane & 15) >> 2, p = lane & 3;
#pragma unroll
    for (int s = 0; s < 2; ++s) {
      bf16x8 af[MT], bfr[NT];
#pragma unroll
      for (int c = 0; c < NT; ++c) {
        int u = (wn * WNT + 16 * c) >> 4;
        int k0 = 32 * s + 8 * g + q;
        int k1 = k0 + 4;
        bf16x4 lo = __builtin_amdgcn_ds_read_tr16_b64_v4i16(
            (lds_b4_ptr)(buf + A_BYTES + k0 * B_ROWB + 32 * (u ^ bswz(k0)) + 8 * p));
        bf16x4 hi = __builtin_amdgcn_ds_read_tr16_b64_v4i16(
            (lds_b4_ptr)(buf + A_BYTES + k1 * B_ROWB + 32 * (u ^ bswz(k1)) + 8 * p));
        bfr[c] = __builtin_shufflevector(lo, hi, 0, 1, 2, 3, 4, 5, 6, 7);
      }
#pragma unroll
      for (int i = 0; i < MT; ++i) {
        int row = wm * WMT + 16 * i + fr;
        int ch = 4 * s + g;
        af[i] = *(const bf16x8*)(buf + row * 128 + 16 * (ch ^ ((row >> 1) & 7)));
      }
#pragma unroll
      for (int i = 0; i < MT; ++i)
#pragma unroll
        for (int c = 0; c < NT; ++c)
          acc[i][c] = __builtin_amdgcn_mfma_f32_16x16x32_bf16(bfr[c], af[i], acc[i][c], 0, 0, 0);
    }
  }
};

typedef Gemm<256, 256, 2, 4> G1;

__global__ __launch_bounds__(512, 2) void k1_gemm(const unsigned short* __restrict__ x,
                                                  const unsigned short* __restrict__ wqkv,
                                                  const unsigned short* __restrict__ w1,
                                                  unsigned short* __restrict__ qkv,
                                                  unsigned short* __restrict__ h) {
  extern __shared__ __attribute__((aligned(16))) char smem[];
  const int tid = threadIdx.x, wave = __builtin_amdgcn_readfirstlane(tid >> 6), lane = tid & 63;
  const int wm = wave >> 2, wn = wave & 3;
  int bid = blockIdx.x;
  const unsigned short* B;
  int ldb, n0, m0;
  bool is_h;
  {
    int xcd = bid & 7, idx = bid >> 3;
    bid = xcd * 25 + idx;
  }
  {
    int ntile = bid >> 3;
    m0 = (bid & 7) * 256;
    if (ntile < 18) { is_h = true; B = w1; ldb = DFF; n0 = ntile * 256; }
    else { is_h = false; B = wqkv; ldb = NQKV; n0 = (ntile - 18) * 256; }
  }

  f32x4 acc[G1::MT][G1::NT];
#pragma unroll
  for (int i = 0; i < G1::MT; ++i)
#pragma unroll
    for (int c = 0; c < G1::NT; ++c) acc[i][c] = (f32x4){0.f, 0.f, 0.f, 0.f};

  unsigned aoff[G1::A_INS], boff[G1::B_INS];
  G1::offsets(aoff, boff, D_, ldb, ldb - n0, wave, lane);
  const unsigned lds0 = (unsigned)(size_t)smem;
  const unsigned short* Ab = x + (long)m0 * D_;
  const unsigned short* Bb = B + n0;
  const int nt = D_ / 64;
  G1::stage(lds0, Ab, Bb, aoff, boff, wave);
  for (int t = 0; t < nt; ++t) {
    WAIT_VM(0);
    __builtin_amdgcn_s_barrier();
    if (t + 1 < nt)
      G1::stage(lds0 + ((t + 1) & 1) * G1::STAGE_BYTES, Ab + (t + 1) * 64, Bb + (long)(t + 1) * 64 * ldb, aoff, boff, wave);
    G1::compute(smem + (t & 1) * G1::STAGE_BYTES, acc, wm, wn, lane);
  }
  __builtin_amdgcn_s_barrier();
  char* wbuf = smem + wave * 16384;
  const int fr = lane & 15, g = lane >> 4;
#pragma unroll
  for (int i = 0; i < G1::MT; ++i)
#pragma unroll
    for (int c = 0; c < G1::NT; ++c) {
      int row = 16 * i + fr, col = 16 * c + 4 * g;
      int gcol = n0 + wn * 64 + col;
      bf16x4 v;
#pragma unroll
      for (int r = 0; r < 4; ++r) {
        float t = acc[i][c][r];
        if (is_h) t = fmaxf(t, 0.f);
        else if (gcol < 2 * D_) t = (t > 0.f ? t + 1.f : __expf(t)) + 1e-6f;
        v[r] = (short)f2bf(t);
      }
      *(bf16x4*)(wbuf + row * 128 + (((col >> 3) ^ (row & 7)) << 4) + ((col & 4) << 1)) = v;
    }
  WAIT_LGKM0;
  unsigned short* dst = is_h ? h : qkv;
#pragma unroll
  for (int it = 0; it < 16; ++it) {
    int row = it * 8 + (lane >> 3), ch = lane & 7;
    bf16x8 v = *(const bf16x8*)(wbuf + row * 128 + ((ch ^ (row & 7)) << 4));
    int gcol = n0 + wn * 64 + ch * 8;
    if (gcol < ldb) *(bf16x8*)(dst + (long)(m0 + wm * 128 + row) * ldb + gcol) = v;
  }
}

#define KVN (DH * DH + DH)
__global__ __launch_bounds__(256) void k2a_kv(const unsigned short* __restrict__ qkv, float* __restrict__ kvpart) {
  __shared__ float red[14][DH][DH + 1];
  const int head = blockIdx.x & 31, chunk = blockIdx.x >> 5;
  const int tid = threadIdx.x;
  const int rg = tid / DH, d = tid % DH;
  if (rg < 14) {
    float acc[DH + 1];
#pragma unroll
    for (int e = 0; e <= DH; ++e) acc[e] = 0.f;
    for (int r = rg; r < 256; r += 14) {
      const unsigned short* row = qkv + (long)(chunk * 256 + r) * NQKV + head * DH;
      float pk = bf2f(row[D_ + d]);
      const unsigned* vp = (const unsigned*)(row + 2 * D_);
#pragma unroll
      for (int e2 = 0; e2 < DH / 2; ++e2) {
        unsigned w = vp[e2];
        acc[2 * e2] += pk * __uint_as_float(w << 16);
        acc[2 * e2 + 1] += pk * __uint_as_float(w & 0xffff0000u);
      }
      acc[DH] += pk;
    }
#pragma unroll
    for (int e = 0; e <= DH; ++e) red[rg][d][e] = acc[e];
  }
  __syncthreads();
  for (int i = tid; i < KVN; i += 256) {
    int dd, e;
    if (i < DH * DH) { dd = i / DH; e = i % DH; } else { dd = i - DH * DH; e = DH; }
    float sum = 0.f;
#pragma unroll
    for (int k = 0; k < 14; ++k) sum += red[k][dd][e];
    kvpart[((long)chunk * NH + head) * KVN + i] = sum;
  }
}

__global__ __launch_bounds__(256) void k2b_o(const unsigned short* __restrict__ qkv, const float* __restrict__ kvpart,
                                             const unsigned short* __restrict__ x, const unsigned short* __restrict__ gate_w,
                                             unsigned short* __restrict__ o, float* __restrict__ gbuf) {
  __shared__ __attribute__((aligned(16))) float kv[4][DH + 1][20];
  __shared__ float gs[64];
  const int rb = blockIdx.x >> 3, hg = blockIdx.x & 7;
  const int tid = threadIdx.x;
  for (int i = tid; i < 4 * KVN; i += 256) {
    int hh = i / KVN, j = i % KVN;
    float sum = 0.f;
#pragma unroll
    for (int c = 0; c < 8; ++c) sum += kvpart[((long)c * NH + hg * 4 + hh) * KVN + j];
    if (j < DH * DH) kv[hh][j / DH][j % DH] = sum;
    else kv[hh][DH][j - DH * DH] = sum;
  }
  {
    int r = tid >> 2, part = tid & 3;
    const bf16x8* xr = (const bf16x8*)(x + (long)(rb * 64 + r) * D_ + part * 144);
    const bf16x8* gw = (const bf16x8*)(gate_w + part * 144);
    float sum = 0.f;
#pragma unroll
    for (int i = 0; i < 18; ++i) {
      bf16x8 a = xr[i], b = gw[i];
#pragma unroll
      for (int j = 0; j < 8; ++j) sum += bf2f((unsigned short)a[j]) * bf2f((unsigned short)b[j]);
    }
    sum += __shfl_xor(sum, 1);
    sum += __shfl_xor(sum, 2);
    float gg = 1.f / (1.f + __expf(-sum));
    if (part == 0) {
      gs[r] = gg;
      if (hg == 0) gbuf[rb * 64 + r] = gg;
    }
  }
  __syncthreads();
  {
    int r = tid & 63, hh = tid >> 6;
    const unsigned* qr = (const unsigned*)(qkv + (long)(rb * 64 + r) * NQKV + (hg * 4 + hh) * DH);
    float pq[DH];
#pragma unroll
    for (int d2 = 0; d2 < DH / 2; ++d2) {
      unsigned w = qr[d2];
      pq[2 * d2] = __uint_as_float(w << 16);
      pq[2 * d2 + 1] = __uint_as_float(w & 0xffff0000u);
    }
    float num[20];
#pragma unroll
    for (int e = 0; e < 20; ++e) num[e] = 0.f;
    float den = 1e-6f;
#pragma unroll
    for (int d = 0; d < DH; ++d) {
      den += pq[d] * kv[hh][DH][d];
#pragma unroll
      for (int e4 = 0; e4 < 5; ++e4) {
        f32x4 kk = *(const f32x4*)&kv[hh][d][4 * e4];
#pragma unroll
        for (int j = 0; j < 4; ++j) num[4 * e4 + j] += pq[d] * kk[j];
      }
    }
    float sc = gs[r] / den;
    unsigned* orow = (unsigned*)(o + (long)(rb * 64 + r) * D_ + (hg * 4 + hh) * DH);
#pragma unroll
    for (int e2 = 0; e2 < DH / 2; ++e2)
      orow[e2] = (unsigned)f2bf(num[2 * e2] * sc) | ((unsigned)f2bf(num[2 * e2 + 1] * sc) << 16);
  }
}

struct G3 {
  static constexpr int A_BYTES = 128 * 128, P_BYTES = 64 * 128, B_BYTES = 3 * P_BYTES;
  static constexpr int STAGE_BYTES = A_BYTES + B_BYTES;
  static constexpr int MT = 4, NT = 3;
  static __device__ __forceinline__ int bswz(int k) { return ((k >> 1) & 1) | (((k >> 3) & 1) << 1); }
  static __device__ __forceinline__ void offsets(unsigned (&aoff)[2], unsigned (&boff)[3], int lda, int wave, int lane) {
#pragma unroll
    for (int i = 0; i < 2; ++i) {
      int off = (wave * 2 + i) * 1024 + lane * 16;
      int row = off >> 7, slot = (off & 127) >> 4;
      int ch = slot ^ ((row >> 1) & 7);
      aoff[i] = (unsigned)(row * lda + ch * 8) * 2u;
    }
#pragma unroll
    for (int i = 0; i < 3; ++i) {
      int off = wave * 1024 + lane * 16;
      int krow = off >> 7, slot = (off & 127) >> 4;
      int ch = slot ^ (bswz(krow) << 1);
      boff[i] = (unsigned)(krow * D_ + i * 64 + ch * 8) * 2u;
    }
  }
  static __device__ __forceinline__ void stage(unsigned lds, const unsigned short* Abase, const unsigned short* Bbase,
                                               const unsigned (&aoff)[2], const unsigned (&boff)[3], int wave) {
#pragma unroll
    for (int i = 0; i < 2; ++i) glds16(aoff[i], Abase, lds + (wave * 2 + i) * 1024);
#pragma unroll
    for (int i = 0; i < 3; ++i) glds16(boff[i], Bbase, lds + A_BYTES + i * P_BYTES + wave * 1024);
  }
  static __device__ __forceinline__ void compute(const char* buf, f32x4 (&acc)[MT][NT], int wm, int wn, int lane) {
    const int fr = lane & 15, g = lane >> 4, q = (lane & 15) >> 2, p = lane & 3;
#pragma unroll
    for (int s = 0; s < 2; ++s) {
      bf16x8 af[MT], bfr[NT];
#pragma unroll
      for (int c = 0; c < NT; ++c) {
        int u16 = wn * 3 + c;
        const char* pb = buf + A_BYTES + (u16 >> 2) * P_BYTES;
        int u = u16 & 3;
        int k0 = 32 * s + 8 * g + q;
        int k1 = k0 + 4;
        bf16x4 lo = __builtin_amdgcn_ds_read_tr16_b64_v4i16((lds_b4_ptr)(pb + k0 * 128 + 32 * (u ^ bswz(k0)) + 8 * p));
        bf16x4 hi = __builtin_amdgcn_ds_read_tr16_b64_v4i16((lds_b4_ptr)(pb + k1 * 128 + 32 * (u ^ bswz(k1)) + 8 * p));
        bfr[c] = __builtin_shufflevector(lo, hi, 0, 1, 2, 3, 4, 5, 6, 7);
      }
#pragma unroll
      for (int i = 0; i < MT; ++i) {
        int row = wm * 64 + 16 * i + fr;
        int ch = 4 * s + g;
        af[i] = *(const bf16x8*)(buf + row * 128 + 16 * (ch ^ ((row >> 1) & 7)));
      }
#pragma unroll
      for (int i = 0; i < MT; ++i)
#pragma unroll
        for (int c = 0; c < NT; ++c)
          acc[i][c] = __builtin_amdgcn_mfma_f32_16x16x32_bf16(bfr[c], af[i], acc[i][c], 0, 0, 0);
    }
  }
};
#define NSPLIT 5

__global__ __launch_bounds__(512, 2) void k3_gemm(const unsigned short* __restrict__ o, const unsigned short* __restrict__ h,
                                                  const unsigned short* __restrict__ wo, const unsigned short* __restrict__ w2,
                                                  const float* __restrict__ gbuf, float* __restrict__ slabs) {
  extern __shared__ __attribute__((aligned(16))) char smem[];
  const int tid = threadIdx.x, wave = __builtin_amdgcn_readfirstlane(tid >> 6), lane = tid & 63;
  const int wm = wave >> 2, wn = wave & 3;
  const int xcd = blockIdx.x & 7, idx = blockIdx.x >> 3;
  const int grp = xcd * 10 + idx / 3;
  const int split = grp % NSPLIT;
  const int n0 = (idx % 3) * 192, m0 = (grp / NSPLIT) * 128;
  const int fr = lane & 15, g = lane >> 4;

  f32x4 acc[G3::MT][G3::NT];
#pragma unroll
  for (int i = 0; i < G3::MT; ++i)
#pragma unroll
    for (int c = 0; c < G3::NT; ++c) acc[i][c] = (f32x4){0.f, 0.f, 0.f, 0.f};

  const int nb = (split == 0) ? 8 : 16;
  const int b0 = (split == 0) ? 0 : 8 + (split - 1) * 16;
  const int nt = (split == 0) ? 17 : 16;

  unsigned hoff[2], woff[3], ooff[2], woff2[3];
  G3::offsets(hoff, woff, DFF, wave, lane);
  G3::offsets(ooff, woff2, D_, wave, lane);
  const unsigned lds0 = (unsigned)(size_t)smem;

  auto do_stage = [&](int t, unsigned lds) {
    if (t < nb) {
      int kt = b0 + t;
      G3::stage(lds, h + (long)m0 * DFF + kt * 64, w2 + (long)kt * 64 * D_ + n0, hoff, woff, wave);
    } else {
      int kt = t - nb;
      G3::stage(lds, o + (long)m0 * D_ + kt * 64, wo + (long)kt * 64 * D_ + n0, ooff, woff, wave);
    }
  };
  auto scale_acc = [&]() {
#pragma unroll
    for (int i = 0; i < G3::MT; ++i) {
      float sc = 1.f - gbuf[m0 + wm * 64 + 16 * i + fr];
#pragma unroll
      for (int c = 0; c < G3::NT; ++c) acc[i][c] *= sc;
    }
  };

  do_stage(0, lds0);
  do_stage(1, lds0 + G3::STAGE_BYTES);
  do_stage(2, lds0 + 2 * G3::STAGE_BYTES);
  int rs = 0;
  for (int t = 0; t < nt; ++t) {
    if (t + 2 < nt) { WAIT_VM(10); } else if (t + 1 < nt) { WAIT_VM(5); } else { WAIT_VM(0); }
    __builtin_amdgcn_s_barrier();
    int ws_ = (rs + 3) & 3;
    if (t + 3 < nt) do_stage(t + 3, lds0 + ws_ * G3::STAGE_BYTES);
    G3::compute(smem + rs * G3::STAGE_BYTES, acc, wm, wn, lane);
    if (t == nb - 1) scale_acc();
    rs = (rs + 1) & 3;
  }
  float* dst = slabs + (long)split * S_ * D_;
#pragma unroll
  for (int i = 0; i < G3::MT; ++i)
#pragma unroll
    for (int c = 0; c < G3::NT; ++c) {
      int row = m0 + wm * 64 + 16 * i + fr, col = n0 + wn * 48 + 16 * c + 4 * g;
      *(f32x4*)(dst + (long)row * D_ + col) = acc[i][c];
    }
}

__global__ __launch_bounds__(256) void k4_reduce(const float* __restrict__ slabs, unsigned short* __restrict__ out) {
  int i = blockIdx.x * 256 + threadIdx.x;
  const f32x4* s0 = (const f32x4*)slabs;
  f32x4 v = s0[i];
#pragma unroll
  for (int k = 1; k < NSPLIT; ++k) v += s0[i + (long)k * S_ * D_ / 4];
  bf16x4 r;
#pragma unroll
  for (int j = 0; j < 4; ++j) r[j] = (short)f2bf(v[j]);
  ((bf16x4*)out)[i] = r;
}

extern "C" void kernel_launch(void* const* d_in, const int* in_sizes, int n_in, void* d_out, int out_size, void* d_ws,
                              size_t ws_size, hipStream_t stream) {
  const unsigned short* x = (const unsigned short*)d_in[0];
  const unsigned short* wqkv = (const unsigned short*)d_in[1];
  const unsigned short* wo = (const unsigned short*)d_in[2];
  const unsigned short* w1 = (const unsigned short*)d_in[4];
  const unsigned short* w2 = (const unsigned short*)d_in[5];
  const unsigned short* gate_w = (const unsigned short*)d_in[6];
  unsigned short* out = (unsigned short*)d_out;

  char* ws = (char*)d_ws;
  unsigned short* qkv = (unsigned short*)ws;
  unsigned short* h = (unsigned short*)(ws + 8u * 1024 * 1024);
  unsigned short* o = (unsigned short*)(ws + 28u * 1024 * 1024);
  float* gbuf = (float*)(ws + 31u * 1024 * 1024);
  float* kvpart = (float*)(ws + 31u * 1024 * 1024 + 65536);
  float* slabs = (float*)(ws + 32u * 1024 * 1024);

  hipFuncSetAttribute((const void*)k1_gemm, hipFuncAttributeMaxDynamicSharedMemorySize, 2 * G1::STAGE_BYTES);
  hipFuncSetAttribute((const void*)k3_gemm, hipFuncAttributeMaxDynamicSharedMemorySize, 4 * G3::STAGE_BYTES);

  k1_gemm<<<144 + 56, 512, 2 * G1::STAGE_BYTES, stream>>>(x, wqkv, w1, qkv, h);
  k2a_kv<<<256, 256, 0, stream>>>(qkv, kvpart);
  k2b_o<<<256, 256, 0, stream>>>(qkv, kvpart, x, gate_w, o, gbuf);
  k3_gemm<<<240, 512, 4 * G3::STAGE_BYTES, stream>>>(o, h, wo, w2, gbuf, slabs);
  k4_reduce<<<S_ * D_ / 4 / 256, 256, 0, stream>>>(slabs, out);
}
```

```cpp
#include <hip/hip_runtime.h>
#include <hip/hip_bf16.h>
#include <stdint.h>

#define S_ 2048
#define D_ 576
#define NQKV 1728
#define DFF 4608
#define NH 32
#define DH 18

typedef __attribute__((ext_vector_type(8))) short bf16x8;
typedef __attribute__((ext_vector_type(4))) short bf16x4;
typedef __attribute__((ext_vector_type(4))) float f32x4;
typedef __attribute__((address_space(3))) bf16x4* lds_b4_ptr;
typedef __attribute__((address_space(3))) void* lds_vptr;

__device__ __forceinline__ unsigned short f2bf(float f) {
  unsigned u = __float_as_uint(f);
  u += 0x7fffu + ((u >> 16) & 1u);
  return (unsigned short)(u >> 16);
}
__device__ __forceinline__ float bf2f(unsigned short h) { return __uint_as_float(((unsigned)h) << 16); }

__device__ __forceinline__ void glds16(unsigned voff, const void* sbase, unsigned lds_dst) {
  unsigned keep;
  asm volatile("s_mov_b32 %0, m0\n\ts_mov_b32 m0, %3\n\ts_nop 0\n\tglobal_load_lds_dwordx4 %1, %2\n\ts_mov_b32 m0, %0"
               : "=&s"(keep) : "v"(voff), "s"(sbase), "s"(lds_dst) : "memory");
}
#define WAIT_VM(n) asm volatile("s_waitcnt vmcnt(" #n ")" ::: "memory")
#define WAIT_LGKM0 asm volatile("s_waitcnt lgkmcnt(0)" ::: "memory")

template <int BM, int BN, int WAVES_M, int WAVES_N>
struct Gemm {
  static constexpr int WMT = BM / WAVES_M, WNT = BN / WAVES_N;
  static constexpr int MT = WMT / 16, NT = WNT / 16;
  static constexpr int A_BYTES = BM * 128, B_ROWB = BN * 2, B_BYTES = 64 * B_ROWB;
  static constexpr int STAGE_BYTES = A_BYTES + B_BYTES;
  static constexpr int A_INS = A_BYTES / 1024 / 8, B_INS = B_BYTES / 1024 / 8;
  static constexpr int UPR = BN / 16;

  static __device__ __forceinline__ int bswz(int k) {
    if (UPR >= 8) return (k & 3) | (((k >> 3) & 1) << 2);
    else return ((k >> 1) & 1) | (((k >> 3) & 1) << 1);
  }
  static __device__ __forceinline__ void offsets(unsigned (&aoff)[A_INS], unsigned (&boff)[B_INS], int lda, int ldb,
                                                 int ncols_left, int wave, int lane) {
#pragma unroll
    for (int i = 0; i < A_INS; ++i) {
      int off = (wave * A_INS + i) * 1024 + lane * 16;
      int row = off >> 7, slot = (off & 127) >> 4;
      int ch = slot ^ ((row >> 1) & 7);
      aoff[i] = (unsigned)(row * lda + ch * 8) * 2u;
    }
#pragma unroll
    for (int i = 0; i < B_INS; ++i) {
      int off = (wave * B_INS + i) * 1024 + lane * 16;
      int krow = off / B_ROWB, slot = (off % B_ROWB) >> 4;
      int ch = slot ^ (bswz(krow) << 1);
      int col = ch * 8;
      if (col > ncols_left - 8) col = ncols_left - 8;
      boff[i] = (unsigned)(krow * ldb + col) * 2u;
    }
  }
  static __device__ __forceinline__ void stage(unsigned lds, const unsigned short* Abase, const unsigned short* Bbase,
                                               const unsigned (&aoff)[A_INS], const unsigned (&boff)[B_INS], int wave) {
#pragma unroll
    for (int i = 0; i < A_INS; ++i) glds16(aoff[i], Abase, lds + (wave * A_INS + i) * 1024);
#pragma unroll
    for (int i = 0; i < B_INS; ++i) glds16(boff[i], Bbase, lds + A_BYTES + (wave * B_INS + i) * 1024);
  }

  static __device__ __forceinline__ void rd(const char* buf, int s, bf16x8 (&af)[MT], bf16x8 (&bfr)[NT], int wm, int wn, int lane) {
    const int fr = lane & 15, g = lane >> 4, q = (lane & 15) >> 2, p = lane & 3;
#pragma unroll
    for (int c = 0; c < NT; ++c) {
      int u = (wn * WNT + 16 * c) >> 4;
      int k0 = 32 * s + 8 * g + q;
      int k1 = k0 + 4;
      bf16x4 lo = __builtin_amdgcn_ds_read_tr16_b64_v4i16(
          (lds_b4_ptr)(buf + A_BYTES + k0 * B_ROWB + 32 * (u ^ bswz(k0)) + 8 * p));
      bf16x4 hi = __builtin_amdgcn_ds_read_tr16_b64_v4i16(
          (lds_b4_ptr)(buf + A_BYTES + k1 * B_ROWB + 32 * (u ^ bswz(k1)) + 8 * p));
      bfr[c] = __builtin_shufflevector(lo, hi, 0, 1, 2, 3, 4, 5, 6, 7);
    }
#pragma unroll
    for (int i = 0; i < MT; ++i) {
      int row = wm * WMT + 16 * i + fr;
      int ch = 4 * s + g;
      af[i] = *(const bf16x8*)(buf + row * 128 + 16 * (ch ^ ((row >> 1) & 7)));
    }
    __builtin_amdgcn_sched_barrier(0);
  }
  static __device__ __forceinline__ void mma(f32x4 (&acc)[MT][NT], const bf16x8 (&af)[MT], const bf16x8 (&bfr)[NT]) {
#pragma unroll
    for (int i = 0; i < MT; ++i)
#pragma unroll
      for (int c = 0; c < NT; ++c)
        acc[i][c] = __builtin_amdgcn_mfma_f32_16x16x32_bf16(bfr[c], af[i], acc[i][c], 0, 0, 0);
    __builtin_amdgcn_sched_barrier(0);
  }
};

typedef Gemm<256, 256, 2, 4> G1;

__global__ __launch_bounds__(512, 2) void k1_gemm(const unsigned short* __restrict__ x,
                                                  const unsigned short* __restrict__ wqkv,
                                                  const unsigned short* __restrict__ w1,
                                                  unsigned short* __restrict__ qkv,
                                                  unsigned short* __restrict__ h) {
  extern __shared__ __attribute__((aligned(16))) char smem[];
  const int tid = threadIdx.x, wave = __builtin_amdgcn_readfirstlane(tid >> 6), lane = tid & 63;
  const int wm = wave >> 2, wn = wave & 3;
  int bid = blockIdx.x;
  const unsigned short* B;
  int ldb, n0, m0;
  bool is_h;
  {
    int xcd = bid & 7, idx = bid >> 3;
    bid = xcd * 25 + idx;
  }
  {
    int ntile = bid >> 3;
    m0 = (bid & 7) * 256;
    if (ntile < 18) { is_h = true; B = w1; ldb = DFF; n0 = ntile * 256; }
    else { is_h = false; B = wqkv; ldb = NQKV; n0 = (ntile - 18) * 256; }
  }

  f32x4 acc[G1::MT][G1::NT];
#pragma unroll
  for (int i = 0; i < G1::MT; ++i)
#pragma unroll
    for (int c = 0; c < G1::NT; ++c) acc[i][c] = (f32x4){0.f, 0.f, 0.f, 0.f};

  unsigned aoff[G1::A_INS], boff[G1::B_INS];
  G1::offsets(aoff, boff, D_, ldb, ldb - n0, wave, lane);
  const unsigned lds0 = (unsigned)(size_t)smem;
  const unsigned short* Ab = x + (long)m0 * D_;
  const unsigned short* Bb = B + n0;
  const int nt = D_ / 64;
  G1::stage(lds0, Ab, Bb, aoff, boff, wave);
  G1::stage(lds0 + G1::STAGE_BYTES, Ab + 64, Bb + (long)64 * ldb, aoff, boff, wave);
  bf16x8 a0[G1::MT], b0[G1::NT], a1[G1::MT], b1[G1::NT];
  WAIT_VM(8);
  __builtin_amdgcn_s_barrier();
  G1::rd(smem, 0, a0, b0, wm, wn, lane);
  for (int t = 0; t < nt; ++t) {
    const char* cur = smem + (t & 1) * G1::STAGE_BYTES;
    G1::rd(cur, 1, a1, b1, wm, wn, lane);
    G1::mma(acc, a0, b0);
    if (t + 1 < nt) {
      WAIT_LGKM0;
      WAIT_VM(0);
      __builtin_amdgcn_s_barrier();
      if (t + 2 < nt)
        G1::stage(lds0 + (t & 1) * G1::STAGE_BYTES, Ab + (t + 2) * 64, Bb + (long)(t + 2) * 64 * ldb, aoff, boff, wave);
      G1::rd(smem + ((t + 1) & 1) * G1::STAGE_BYTES, 0, a0, b0, wm, wn, lane);
    }
    G1::mma(acc, a1, b1);
  }
  __builtin_amdgcn_s_barrier();
  char* wbuf = smem + wave * 16384;
  const int fr = lane & 15, g = lane >> 4;
#pragma unroll
  for (int i = 0; i < G1::MT; ++i)
#pragma unroll
    for (int c = 0; c < G1::NT; ++c) {
      int row = 16 * i + fr, col = 16 * c + 4 * g;
      int gcol = n0 + wn * 64 + col;
      bf16x4 v;
#pragma unroll
      for (int r = 0; r < 4; ++r) {
        float t = acc[i][c][r];
        if (is_h) t = fmaxf(t, 0.f);
        else if (gcol < 2 * D_) t = (t > 0.f ? t + 1.f : __expf(t)) + 1e-6f;
        v[r] = (short)f2bf(t);
      }
      *(bf16x4*)(wbuf + row * 128 + (((col >> 3) ^ (row & 7)) << 4) + ((col & 4) << 1)) = v;
    }
  WAIT_LGKM0;
  unsigned short* dst = is_h ? h : qkv;
#pragma unroll
  for (int it = 0; it < 16; ++it) {
    int row = it * 8 + (lane >> 3), ch = lane & 7;
    bf16x8 v = *(const bf16x8*)(wbuf + row * 128 + ((ch ^ (row & 7)) << 4));
    int gcol = n0 + wn * 64 + ch * 8;
    if (gcol < ldb) *(bf16x8*)(dst + (long)(m0 + wm * 128 + row) * ldb + gcol) = v;
  }
}

#define KVN (DH * DH + DH)
#define NCH 16
__global__ __launch_bounds__(256) void k2a_kv(const unsigned short* __restrict__ qkv, float* __restrict__ kvpart) {
  __shared__ __attribute__((aligned(16))) char sm[4 * 80 * 80 * 4];
  char* kimg = sm;
  char* vimg = sm + 128 * 160;
  const int hg = blockIdx.x & 7, chunk = blockIdx.x >> 3;
  const int tid = threadIdx.x, wave = tid >> 6, lane = tid & 63;
  {
    bf16x8 r[10];
#pragma unroll
    for (int i = 0; i < 10; ++i) {
      int idx = tid + i * 256;
      int which = idx / 1280, rem = idx % 1280, row = rem / 10, c = rem % 10;
      if (c < 9) r[i] = *(const bf16x8*)(qkv + (long)(chunk * 128 + row) * NQKV + (1 + which) * D_ + hg * 72 + c * 8);
      else { r[i] = (bf16x8){0, 0, 0, 0, 0, 0, 0, 0}; if (which) r[i][0] = (short)0x3f80; }
    }
#pragma unroll
    for (int i = 0; i < 10; ++i) {
      int idx = tid + i * 256;
      int which = idx / 1280, rem = idx % 1280, row = rem / 10, c = rem % 10;
      *(bf16x8*)((which ? vimg : kimg) + row * 160 + c * 16) = r[i];
    }
  }
  __syncthreads();
  f32x4 acc[5][5];
  {
    const int g = lane >> 4, q = (lane & 15) >> 2, p = lane & 3;
    const int r0 = wave * 32 + 8 * g + q;
    bf16x8 kf[5], vf[5];
#pragma unroll
    for (int c = 0; c < 5; ++c) {
      bf16x4 lo = __builtin_amdgcn_ds_read_tr16_b64_v4i16((lds_b4_ptr)(kimg + r0 * 160 + c * 32 + p * 8));
      bf16x4 hi = __builtin_amdgcn_ds_read_tr16_b64_v4i16((lds_b4_ptr)(kimg + (r0 + 4) * 160 + c * 32 + p * 8));
      kf[c] = __builtin_shufflevector(lo, hi, 0, 1, 2, 3, 4, 5, 6, 7);
      lo = __builtin_amdgcn_ds_read_tr16_b64_v4i16((lds_b4_ptr)(vimg + r0 * 160 + c * 32 + p * 8));
      hi = __builtin_amdgcn_ds_read_tr16_b64_v4i16((lds_b4_ptr)(vimg + (r0 + 4) * 160 + c * 32 + p * 8));
      vf[c] = __builtin_shufflevector(lo, hi, 0, 1, 2, 3, 4, 5, 6, 7);
    }
#pragma unroll
    for (int a = 0; a < 5; ++a)
#pragma unroll
      for (int b = 0; b < 5; ++b)
        acc[a][b] = __builtin_amdgcn_mfma_f32_16x16x32_bf16(kf[a], vf[b], (f32x4){0.f, 0.f, 0.f, 0.f}, 0, 0, 0);
  }
  __syncthreads();
  float* red = (float*)sm + wave * 6400;
#pragma unroll
  for (int a = 0; a < 5; ++a)
#pragma unroll
    for (int b = 0; b < 5; ++b)
#pragma unroll
      for (int r = 0; r < 4; ++r) red[(16 * a + 4 * (lane >> 4) + r) * 80 + 16 * b + (lane & 15)] = acc[a][b][r];
  __syncthreads();
  const float* all = (const float*)sm;
  for (int i = tid; i < 4 * KVN; i += 256) {
    int hh = i / KVN, j = i % KVN;
    int d, e;
    if (j < DH * DH) { d = hh * DH + j / DH; e = hh * DH + j % DH; } else { d = hh * DH + j - DH * DH; e = 72; }
    float sum = all[d * 80 + e] + all[6400 + d * 80 + e] + all[12800 + d * 80 + e] + all[19200 + d * 80 + e];
    kvpart[((long)chunk * NH + hg * 4 + hh) * KVN + j] = sum;
  }
}

__global__ __launch_bounds__(256) void k2b_o(const unsigned short* __restrict__ qkv, const float* __restrict__ kvpart,
                                             const unsigned short* __restrict__ x, const unsigned short* __restrict__ gate_w,
                                             unsigned short* __restrict__ o, float* __restrict__ gbuf) {
  __shared__ __attribute__((aligned(16))) float kv[4][DH + 1][20];
  __shared__ float gs[64];
  const int rb = blockIdx.x >> 3, hg = blockIdx.x & 7;
  const int tid = threadIdx.x;
  {
    float part[6][NCH];
#pragma unroll
    for (int it = 0; it < 6; ++it) {
      int i = tid + it * 256;
      if (i >= 4 * KVN) i = 4 * KVN - 1;
      int hh = i / KVN, j = i % KVN;
#pragma unroll
      for (int c = 0; c < NCH; ++c) part[it][c] = kvpart[((long)c * NH + hg * 4 + hh) * KVN + j];
    }
#pragma unroll
    for (int it = 0; it < 6; ++it) {
      int i = tid + it * 256;
      if (i < 4 * KVN) {
        int hh = i / KVN, j = i % KVN;
        float sum = 0.f;
#pragma unroll
        for (int c = 0; c < NCH; ++c) sum += part[it][c];
        if (j < DH * DH) kv[hh][j / DH][j % DH] = sum;
        else kv[hh][DH][j - DH * DH] = sum;
      }
    }
  }
  {
    int r = tid >> 2, part = tid & 3;
    const bf16x8* xr = (const bf16x8*)(x + (long)(rb * 64 + r) * D_ + part * 144);
    const bf16x8* gw = (const bf16x8*)(gate_w + part * 144);
    float sum = 0.f;
#pragma unroll
    for (int i = 0; i < 18; ++i) {
      bf16x8 a = xr[i], b = gw[i];
#pragma unroll
      for (int j = 0; j < 8; ++j) sum += bf2f((unsigned short)a[j]) * bf2f((unsigned short)b[j]);
    }
    sum += __shfl_xor(sum, 1);
    sum += __shfl_xor(sum, 2);
    float gg = 1.f / (1.f + __expf(-sum));
    if (part == 0) {
      gs[r] = gg;
      if (hg == 0) gbuf[rb * 64 + r] = gg;
    }
  }
  __syncthreads();
  {
    int r = tid & 63, hh = tid >> 6;
    const unsigned* qr = (const unsigned*)(qkv + (long)(rb * 64 + r) * NQKV + (hg * 4 + hh) * DH);
    float pq[DH];
#pragma unroll
    for (int d2 = 0; d2 < DH / 2; ++d2) {
      unsigned w = qr[d2];
      pq[2 * d2] = __uint_as_float(w << 16);
      pq[2 * d2 + 1] = __uint_as_float(w & 0xffff0000u);
    }
    float num[20];
#pragma unroll
    for (int e = 0; e < 20; ++e) num[e] = 0.f;
    float den = 1e-6f;
#pragma unroll
    for (int d = 0; d < DH; ++d) {
      den += pq[d] * kv[hh][DH][d];
#pragma unroll
      for (int e4 = 0; e4 < 5; ++e4) {
        f32x4 kk = *(const f32x4*)&kv[hh][d][4 * e4];
#pragma unroll
        for (int j = 0; j < 4; ++j) num[4 * e4 + j] += pq[d] * kk[j];
      }
    }
    float sc = gs[r] / den;
    unsigned* orow = (unsigned*)(o + (long)(rb * 64 + r) * D_ + (hg * 4 + hh) * DH);
#pragma unroll
    for (int e2 = 0; e2 < DH / 2; ++e2)
      orow[e2] = (unsigned)f2bf(num[2 * e2] * sc) | ((unsigned)f2bf(num[2 * e2 + 1] * sc) << 16);
  }
}

struct G3 {
  static constexpr int A_BYTES = 128 * 128, P_BYTES = 64 * 128, B_BYTES = 3 * P_BYTES;
  static constexpr int STAGE_BYTES = A_BYTES + B_BYTES;
  static constexpr int MT = 4, NT = 3;
  static __device__ __forceinline__ int bswz(int k) { return ((k >> 1) & 1) | (((k >> 3) & 1) << 1); }
  static __device__ __forceinline__ void offsets(unsigned (&aoff)[2], unsigned (&boff)[3], int lda, int wave, int lane) {
#pragma unroll
    for (int i = 0; i < 2; ++i) {
      int off = (wave * 2 + i) * 1024 + lane * 16;
      int row = off >> 7, slot = (off & 127) >> 4;
      int ch = slot ^ ((row >> 1) & 7);
      aoff[i] = (unsigned)(row * lda + ch * 8) * 2u;
    }
#pragma unroll
    for (int i = 0; i < 3; ++i) {
      int off = wave * 1024 + lane * 16;
      int krow = off >> 7, slot = (off & 127) >> 4;
      int ch = slot ^ (bswz(krow) << 1);
      boff[i] = (unsigned)(krow * D_ + i * 64 + ch * 8) * 2u;
    }
  }
  static __device__ __forceinline__ void stage(unsigned lds, const unsigned short* Abase, const unsigned short* Bbase,
                                               const unsigned (&aoff)[2], const unsigned (&boff)[3], int wave) {
#pragma unroll
    for (int i = 0; i < 2; ++i) glds16(aoff[i], Abase, lds + (wave * 2 + i) * 1024);
#pragma unroll
    for (int i = 0; i < 3; ++i) glds16(boff[i], Bbase, lds + A_BYTES + i * P_BYTES + wave * 1024);
  }
  static __device__ __forceinline__ void rd(const char* buf, int s, bf16x8 (&af)[MT], bf16x8 (&bfr)[NT], int wm, int wn, int lane) {
    const int fr = lane & 15, g = lane >> 4, q = (lane & 15) >> 2, p = lane & 3;
#pragma unroll
    for (int c = 0; c < NT; ++c) {
      int u16 = wn * 3 + c;
      const char* pb = buf + A_BYTES + (u16 >> 2) * P_BYTES;
      int u = u16 & 3;
      int k0 = 32 * s + 8 * g + q;
      int k1 = k0 + 4;
      bf16x4 lo = __builtin_amdgcn_ds_read_tr16_b64_v4i16((lds_b4_ptr)(pb + k0 * 128 + 32 * (u ^ bswz(k0)) + 8 * p));
      bf16x4 hi = __builtin_amdgcn_ds_read_tr16_b64_v4i16((lds_b4_ptr)(pb + k1 * 128 + 32 * (u ^ bswz(k1)) + 8 * p));
      bfr[c] = __builtin_shufflevector(lo, hi, 0, 1, 2, 3, 4, 5, 6, 7);
    }
#pragma unroll
    for (int i = 0; i < MT; ++i) {
      int row = wm * 64 + 16 * i + fr;
      int ch = 4 * s + g;
      af[i] = *(const bf16x8*)(buf + row * 128 + 16 * (ch ^ ((row >> 1) & 7)));
    }
    __builtin_amdgcn_sched_barrier(0);
  }
  static __device__ __forceinline__ void mma(f32x4 (&acc)[MT][NT], const bf16x8 (&af)[MT], const bf16x8 (&bfr)[NT]) {
#pragma unroll
    for (int i = 0; i < MT; ++i)
#pragma unroll
      for (int c = 0; c < NT; ++c)
        acc[i][c] = __builtin_amdgcn_mfma_f32_16x16x32_bf16(bfr[c], af[i], acc[i][c], 0, 0, 0);
    __builtin_amdgcn_sched_barrier(0);
  }
};
#define NSPLIT 5

__global__ __launch_bounds__(512, 2) void k3_gemm(const unsigned short* __restrict__ o, const unsigned short* __restrict__ h,
                                                  const unsigned short* __restrict__ wo, const unsigned short* __restrict__ w2,
                                                  const float* __restrict__ gbuf, float* __restrict__ slabs) {
  extern __shared__ __attribute__((aligned(16))) char smem[];
  const int tid = threadIdx.x, wave = __builtin_amdgcn_readfirstlane(tid >> 6), lane = tid & 63;
  const int wm = wave >> 2, wn = wave & 3;
  const int xcd = blockIdx.x & 7, idx = blockIdx.x >> 3;
  const int grp = xcd * 10 + idx / 3;
  const int split = grp % NSPLIT;
  const int n0 = (idx % 3) * 192, m0 = (grp / NSPLIT) * 128;
  const int fr = lane & 15, g = lane >> 4;

  f32x4 acc[G3::MT][G3::NT];
#pragma unroll
  for (int i = 0; i < G3::MT; ++i)
#pragma unroll
    for (int c = 0; c < G3::NT; ++c) acc[i][c] = (f32x4){0.f, 0.f, 0.f, 0.f};

  const int nb = (split == 0) ? 8 : 16;
  const int b0 = (split == 0) ? 0 : 8 + (split - 1) * 16;
  const int nt = (split == 0) ? 17 : 16;

  unsigned hoff[2], woff[3], ooff[2], woff2[3];
  G3::offsets(hoff, woff, DFF, wave, lane);
  G3::offsets(ooff, woff2, D_, wave, lane);
  const unsigned lds0 = (unsigned)(size_t)smem;

  auto do_stage = [&](int t, unsigned lds) {
    if (t < nb) {
      int kt = b0 + t;
      G3::stage(lds, h + (long)m0 * DFF + kt * 64, w2 + (long)kt * 64 * D_ + n0, hoff, woff, wave);
    } else {
      int kt = t - nb;
      G3::stage(lds, o + (long)m0 * D_ + kt * 64, wo + (long)kt * 64 * D_ + n0, ooff, woff, wave);
    }
  };
  auto scale_acc = [&]() {
#pragma unroll
    for (int i = 0; i < G3::MT; ++i) {
      float sc = 1.f - gbuf[m0 + wm * 64 + 16 * i + fr];
#pragma unroll
      for (int c = 0; c < G3::NT; ++c) acc[i][c] *= sc;
    }
  };

  do_stage(0, lds0);
  do_stage(1, lds0 + G3::STAGE_BYTES);
  do_stage(2, lds0 + 2 * G3::STAGE_BYTES);
  bf16x8 fa0[G3::MT], fb0[G3::NT], fa1[G3::MT], fb1[G3::NT];
  WAIT_VM(10);
  __builtin_amdgcn_s_barrier();
  G3::rd(smem, 0, fa0, fb0, wm, wn, lane);
  for (int t = 0; t < nt; ++t) {
    const char* cur = smem + (t & 3) * G3::STAGE_BYTES;
    G3::rd(cur, 1, fa1, fb1, wm, wn, lane);
    G3::mma(acc, fa0, fb0);
    if (t + 1 < nt) {
      WAIT_LGKM0;
      if (t + 2 < nt) { WAIT_VM(5); } else { WAIT_VM(0); }
      __builtin_amdgcn_s_barrier();
      if (t + 3 < nt) do_stage(t + 3, lds0 + ((t + 3) & 3) * G3::STAGE_BYTES);
      G3::rd(smem + ((t + 1) & 3) * G3::STAGE_BYTES, 0, fa0, fb0, wm, wn, lane);
    }
    G3::mma(acc, fa1, fb1);
    if (t == nb - 1) scale_acc();
  }
  float* dst = slabs + (long)split * S_ * D_;
#pragma unroll
  for (int i = 0; i < G3::MT; ++i)
#pragma unroll
    for (int c = 0; c < G3::NT; ++c) {
      int row = m0 + wm * 64 + 16 * i + fr, col = n0 + wn * 48 + 16 * c + 4 * g;
      *(f32x4*)(dst + (long)row * D_ + col) = acc[i][c];
    }
}

__global__ __launch_bounds__(256) void k4_reduce(const float* __restrict__ slabs, unsigned short* __restrict__ out) {
  int i = blockIdx.x * 256 + threadIdx.x;
  const f32x4* s0 = (const f32x4*)slabs;
  f32x4 v = s0[i];
#pragma unroll
  for (int k = 1; k < NSPLIT; ++k) v += s0[i + (long)k * S_ * D_ / 4];
  bf16x4 r;
#pragma unroll
  for (int j = 0; j < 4; ++j) r[j] = (short)f2bf(v[j]);
  ((bf16x4*)out)[i] = r;
}

extern "C" void kernel_launch(void* const* d_in, const int* in_sizes, int n_in, void* d_out, int out_size, void* d_ws,
                              size_t ws_size, hipStream_t stream) {
  const unsigned short* x = (const unsigned short*)d_in[0];
  const unsigned short* wqkv = (const unsigned short*)d_in[1];
  const unsigned short* wo = (const unsigned short*)d_in[2];
  const unsigned short* w1 = (const unsigned short*)d_in[4];
  const unsigned short* w2 = (const unsigned short*)d_in[5];
  const unsigned short* gate_w = (const unsigned short*)d_in[6];
  unsigned short* out = (unsigned short*)d_out;

  char* ws = (char*)d_ws;
  unsigned short* qkv = (unsigned short*)ws;
  unsigned short* h = (unsigned short*)(ws + 8u * 1024 * 1024);
  unsigned short* o = (unsigned short*)(ws + 28u * 1024 * 1024);
  float* gbuf = (float*)(ws + 31u * 1024 * 1024);
  float* kvpart = (float*)(ws + 31u * 1024 * 1024 + 65536);
  float* slabs = (float*)(ws + 32u * 1024 * 1024);

  hipFuncSetAttribute((const void*)k1_gemm, hipFuncAttributeMaxDynamicSharedMemorySize, 2 * G1::STAGE_BYTES);
  hipFuncSetAttribute((const void*)k3_gemm, hipFuncAttributeMaxDynamicSharedMemorySize, 4 * G3::STAGE_BYTES);

  k1_gemm<<<144 + 56, 512, 2 * G1::STAGE_BYTES, stream>>>(x, wqkv, w1, qkv, h);
  k2a_kv<<<128, 256, 0, stream>>>(qkv, kvpart);
  k2b_o<<<256, 256, 0, stream>>>(qkv, kvpart, x, gate_w, o, gbuf);
  k3_gemm<<<240, 512, 4 * G3::STAGE_BYTES, stream>>>(o, h, wo, w2, gbuf, slabs);
  k4_reduce<<<S_ * D_ / 4 / 256, 256, 0, stream>>>(slabs, out);
}
```

```cpp
#include <hip/hip_runtime.h>
#include <hip/hip_bf16.h>
#include <stdint.h>

#define S_ 2048
#define D_ 576
#define NQKV 1728
#define DFF 4608
#define NH 32
#define DH 18

typedef __attribute__((ext_vector_type(8))) short bf16x8;
typedef __attribute__((ext_vector_type(4))) short bf16x4;
typedef __attribute__((ext_vector_type(4))) float f32x4;
typedef __attribute__((address_space(3))) bf16x4* lds_b4_ptr;
typedef __attribute__((address_space(3))) void* lds_vptr;

__device__ __forceinline__ unsigned short f2bf(float f) {
  unsigned u = __float_as_uint(f);
  u += 0x7fffu + ((u >> 16) & 1u);
  return (unsigned short)(u >> 16);
}
__device__ __forceinline__ float bf2f(unsigned short h) { return __uint_as_float(((unsigned)h) << 16); }

__device__ __forceinline__ void glds16(unsigned voff, const void* sbase, unsigned lds_dst) {
  unsigned keep;
  asm volatile("s_mov_b32 %0, m0\n\ts_mov_b32 m0, %3\n\ts_nop 0\n\tglobal_load_lds_dwordx4 %1, %2\n\ts_mov_b32 m0, %0"
               : "=&s"(keep) : "v"(voff), "s"(sbase), "s"(lds_dst) : "memory");
}
#define WAIT_VM(n) asm volatile("s_waitcnt vmcnt(" #n ")" ::: "memory")
#define WAIT_LGKM0 asm volatile("s_waitcnt lgkmcnt(0)" ::: "memory")

template <int BM, int BN, int WAVES_M, int WAVES_N>
struct Gemm {
  static constexpr int WMT = BM / WAVES_M, WNT = BN / WAVES_N;
  static constexpr int MT = WMT / 16, NT = WNT / 16;
  static constexpr int A_BYTES = BM * 128, B_ROWB = BN * 2, B_BYTES = 64 * B_ROWB;
  static constexpr int STAGE_BYTES = A_BYTES + B_BYTES;
  static constexpr int A_INS = A_BYTES / 1024 / 8, B_INS = B_BYTES / 1024 / 8;
  static constexpr int UPR = BN / 16;

  static __device__ __forceinline__ int bswz(int k) {
    if (UPR >= 8) return (k & 3) | (((k >> 3) & 1) << 2);
    else return ((k >> 1) & 1) | (((k >> 3) & 1) << 1);
  }
  static __device__ __forceinline__ void offsets(unsigned (&aoff)[A_INS], unsigned (&boff)[B_INS], int lda, int ldb,
                                                 int ncols_left, int wave, int lane) {
#pragma unroll
    for (int i = 0; i < A_INS; ++i) {
      int off = (wave * A_INS + i) * 1024 + lane * 16;
      int row = off >> 7, slot = (off & 127) >> 4;
      int ch = slot ^ ((row >> 1) & 7);
      aoff[i] = (unsigned)(row * lda + ch * 8) * 2u;
    }
#pragma unroll
    for (int i = 0; i < B_INS; ++i) {
      int off = (wave * B_INS + i) * 1024 + lane * 16;
      int krow = off / B_ROWB, slot = (off % B_ROWB) >> 4;
      int ch = slot ^ (bswz(krow) << 1);
      int col = ch * 8;
      if (col > ncols_left - 8) col = ncols_left - 8;
      boff[i] = (unsigned)(krow * ldb + col) * 2u;
    }
  }
  static __device__ __forceinline__ void stage(unsigned lds, const unsigned short* Abase, const unsigned short* Bbase,
                                               const unsigned (&aoff)[A_INS], const unsigned (&boff)[B_INS], int wave) {
#pragma unroll
    for (int i = 0; i < A_INS; ++i) glds16(aoff[i], Abase, lds + (wave * A_INS + i) * 1024);
#pragma unroll
    for (int i = 0; i < B_INS; ++i) glds16(boff[i], Bbase, lds + A_BYTES + (wave * B_INS + i) * 1024);
  }

  static __device__ __forceinline__ void rd(const char* buf, int s, bf16x8 (&af)[MT], bf16x8 (&bfr)[NT], int wm, int wn, int lane) {
    const int fr = lane & 15, g = lane >> 4, q = (lane & 15) >> 2, p = lane & 3;
#pragma unroll
    for (int c = 0; c < NT; ++c) {
      int u = (wn * WNT + 16 * c) >> 4;
      int k0 = 32 * s + 8 * g + q;
      int k1 = k0 + 4;
      bf16x4 lo = __builtin_amdgcn_ds_read_tr16_b64_v4i16(
          (lds_b4_ptr)(buf + A_BYTES + k0 * B_ROWB + 32 * (u ^ bswz(k0)) + 8 * p));
      bf16x4 hi = __builtin_amdgcn_ds_read_tr16_b64_v4i16(
          (lds_b4_ptr)(buf + A_BYTES + k1 * B_ROWB + 32 * (u ^ bswz(k1)) + 8 * p));
      bfr[c] = __builtin_shufflevector(lo, hi, 0, 1, 2, 3, 4, 5, 6, 7);
    }
#pragma unroll
    for (int i = 0; i < MT; ++i) {
      int row = wm * WMT + 16 * i + fr;
      int ch = 4 * s + g;
      af[i] = *(const bf16x8*)(buf + row * 128 + 16 * (ch ^ ((row >> 1) & 7)));
    }
    __builtin_amdgcn_sched_barrier(0);
  }
  static __device__ __forceinline__ void mma(f32x4 (&acc)[MT][NT], const bf16x8 (&af)[MT], const bf16x8 (&bfr)[NT]) {
#pragma unroll
    for (int i = 0; i < MT; ++i)
#pragma unroll
      for (int c = 0; c < NT; ++c)
        acc[i][c] = __builtin_amdgcn_mfma_f32_16x16x32_bf16(bfr[c], af[i], acc[i][c], 0, 0, 0);
    __builtin_amdgcn_sched_barrier(0);
  }
};

typedef Gemm<256, 256, 2, 4> G1;

__global__ __launch_bounds__(512, 2) void k1_gemm(const unsigned short* __restrict__ x,
                                                  const unsigned short* __restrict__ wqkv,
                                                  const unsigned short* __restrict__ w1,
                                                  unsigned short* __restrict__ qkv,
                                                  unsigned short* __restrict__ h) {
  extern __shared__ __attribute__((aligned(16))) char smem[];
  const int tid = threadIdx.x, wave = __builtin_amdgcn_readfirstlane(tid >> 6), lane = tid & 63;
  const int wm = wave >> 2, wn = wave & 3;
  int bid = blockIdx.x;
  const unsigned short* B;
  int ldb, n0, m0;
  bool is_h;
  {
    int xcd = bid & 7, idx = bid >> 3;
    bid = xcd * 25 + idx;
  }
  {
    int ntile = bid >> 3;
    m0 = (bid & 7) * 256;
    if (ntile < 18) { is_h = true; B = w1; ldb = DFF; n0 = ntile * 256; }
    else { is_h = false; B = wqkv; ldb = NQKV; n0 = (ntile - 18) * 256; }
  }

  f32x4 acc[G1::MT][G1::NT];
#pragma unroll
  for (int i = 0; i < G1::MT; ++i)
#pragma unroll
    for (int c = 0; c < G1::NT; ++c) acc[i][c] = (f32x4){0.f, 0.f, 0.f, 0.f};

  unsigned aoff[G1::A_INS], boff[G1::B_INS];
  G1::offsets(aoff, boff, D_, ldb, ldb - n0, wave, lane);
  const unsigned lds0 = (unsigned)(size_t)smem;
  const unsigned short* Ab = x + (long)m0 * D_;
  const unsigned short* Bb = B + n0;
  const int nt = D_ / 64;
  G1::stage(lds0, Ab, Bb, aoff, boff, wave);
  G1::stage(lds0 + G1::STAGE_BYTES, Ab + 64, Bb + (long)64 * ldb, aoff, boff, wave);
  bf16x8 a0[G1::MT], b0[G1::NT], a1[G1::MT], b1[G1::NT];
  WAIT_VM(8);
  __builtin_amdgcn_s_barrier();
  G1::rd(smem, 0, a0, b0, wm, wn, lane);
  for (int t = 0; t < nt; ++t) {
    const char* cur = smem + (t & 1) * G1::STAGE_BYTES;
    G1::rd(cur, 1, a1, b1, wm, wn, lane);
    G1::mma(acc, a0, b0);
    if (t + 1 < nt) {
      WAIT_LGKM0;
      WAIT_VM(0);
      __builtin_amdgcn_s_barrier();
      if (t + 2 < nt)
        G1::stage(lds0 + (t & 1) * G1::STAGE_BYTES, Ab + (t + 2) * 64, Bb + (long)(t + 2) * 64 * ldb, aoff, boff, wave);
      G1::rd(smem + ((t + 1) & 1) * G1::STAGE_BYTES, 0, a0, b0, wm, wn, lane);
    }
    G1::mma(acc, a1, b1);
  }
  __builtin_amdgcn_s_barrier();
  char* wbuf = smem + wave * 16384;
  const int fr = lane & 15, g = lane >> 4;
#pragma unroll
  for (int i = 0; i < G1::MT; ++i)
#pragma unroll
    for (int c = 0; c < G1::NT; ++c) {
      int row = 16 * i + fr, col = 16 * c + 4 * g;
      int gcol = n0 + wn * 64 + col;
      bf16x4 v;
#pragma unroll
      for (int r = 0; r < 4; ++r) {
        float t = acc[i][c][r];
        if (is_h) t = fmaxf(t, 0.f);
        else if (gcol < 2 * D_) t = (t > 0.f ? t + 1.f : __expf(t)) + 1e-6f;
        v[r] = (short)f2bf(t);
      }
      *(bf16x4*)(wbuf + row * 128 + (((col >> 3) ^ (row & 7)) << 4) + ((col & 4) << 1)) = v;
    }
  WAIT_LGKM0;
  unsigned short* dst = is_h ? h : qkv;
#pragma unroll
  for (int it = 0; it < 16; ++it) {
    int row = it * 8 + (lane >> 3), ch = lane & 7;
    bf16x8 v = *(const bf16x8*)(wbuf + row * 128 + ((ch ^ (row & 7)) << 4));
    int gcol = n0 + wn * 64 + ch * 8;
    if (gcol < ldb) *(bf16x8*)(dst + (long)(m0 + wm * 128 + row) * ldb + gcol) = v;
  }
}

#define KVN (DH * DH + DH)
#define NCH 16
__global__ __launch_bounds__(256) void k2a_kv(const unsigned short* __restrict__ qkv, float* __restrict__ kvpart) {
  __shared__ __attribute__((aligned(16))) char sm[4 * 80 * 80 * 4];
  char* kimg = sm;
  char* vimg = sm + 128 * 160;
  const int hg = blockIdx.x & 7, chunk = blockIdx.x >> 3;
  const int tid = threadIdx.x, wave = tid >> 6, lane = tid & 63;
  {
    bf16x8 r[10];
#pragma unroll
    for (int i = 0; i < 10; ++i) {
      int idx = tid + i * 256;
      int which = idx / 1280, rem = idx % 1280, row = rem / 10, c = rem % 10;
      if (c < 9) r[i] = *(const bf16x8*)(qkv + (long)(chunk * 128 + row) * NQKV + (1 + which) * D_ + hg * 72 + c * 8);
      else { r[i] = (bf16x8){0, 0, 0, 0, 0, 0, 0, 0}; if (which) r[i][0] = (short)0x3f80; }
    }
#pragma unroll
    for (int i = 0; i < 10; ++i) {
      int idx = tid + i * 256;
      int which = idx / 1280, rem = idx % 1280, row = rem / 10, c = rem % 10;
      *(bf16x8*)((which ? vimg : kimg) + row * 160 + c * 16) = r[i];
    }
  }
  __syncthreads();
  f32x4 acc[5][5];
  {
    const int g = lane >> 4, q = (lane & 15) >> 2, p = lane & 3;
    const int r0 = wave * 32 + 8 * g + q;
    bf16x8 kf[5], vf[5];
#pragma unroll
    for (int c = 0; c < 5; ++c) {
      bf16x4 lo = __builtin_amdgcn_ds_read_tr16_b64_v4i16((lds_b4_ptr)(kimg + r0 * 160 + c * 32 + p * 8));
      bf16x4 hi = __builtin_amdgcn_ds_read_tr16_b64_v4i16((lds_b4_ptr)(kimg + (r0 + 4) * 160 + c * 32 + p * 8));
      kf[c] = __builtin_shufflevector(lo, hi, 0, 1, 2, 3, 4, 5, 6, 7);
      lo = __builtin_amdgcn_ds_read_tr16_b64_v4i16((lds_b4_ptr)(vimg + r0 * 160 + c * 32 + p * 8));
      hi = __builtin_amdgcn_ds_read_tr16_b64_v4i16((lds_b4_ptr)(vimg + (r0 + 4) * 160 + c * 32 + p * 8));
      vf[c] = __builtin_shufflevector(lo, hi, 0, 1, 2, 3, 4, 5, 6, 7);
    }
#pragma unroll
    for (int a = 0; a < 5; ++a)
#pragma unroll
      for (int b = 0; b < 5; ++b)
        acc[a][b] = __builtin_amdgcn_mfma_f32_16x16x32_bf16(kf[a], vf[b], (f32x4){0.f, 0.f, 0.f, 0.f}, 0, 0, 0);
  }
  __syncthreads();
  float* red = (float*)sm + wave * 6400;
#pragma unroll
  for (int a = 0; a < 5; ++a)
#pragma unroll
    for (int b = 0; b < 5; ++b)
#pragma unroll
      for (int r = 0; r < 4; ++r) red[(16 * a + 4 * (lane >> 4) + r) * 80 + 16 * b + (lane & 15)] = acc[a][b][r];
  __syncthreads();
  const float* all = (const float*)sm;
  for (int i = tid; i < 4 * KVN; i += 256) {
    int hh = i / KVN, j = i % KVN;
    int d, e;
    if (j < DH * DH) { d = hh * DH + j / DH; e = hh * DH + j % DH; } else { d = hh * DH + j - DH * DH; e = 72; }
    float sum = all[d * 80 + e] + all[6400 + d * 80 + e] + all[12800 + d * 80 + e] + all[19200 + d * 80 + e];
    kvpart[((long)chunk * NH + hg * 4 + hh) * KVN + j] = sum;
  }
}

__global__ __launch_bounds__(256) void k2b_o(const unsigned short* __restrict__ qkv, const float* __restrict__ kvpart,
                                             const unsigned short* __restrict__ x, const unsigned short* __restrict__ gate_w,
                                             unsigned short* __restrict__ o, float* __restrict__ gbuf) {
  __shared__ __attribute__((aligned(16))) float kv[4][DH + 1][20];
  __shared__ float gs[64];
  const int rb = blockIdx.x >> 3, hg = blockIdx.x & 7;
  const int tid = threadIdx.x;
  unsigned qw[DH / 2];
  {
    const unsigned* qr = (const unsigned*)(qkv + (long)(rb * 64 + (tid & 63)) * NQKV + (hg * 4 + (tid >> 6)) * DH);
#pragma unroll
    for (int d2 = 0; d2 < DH / 2; ++d2) qw[d2] = qr[d2];
  }
  {
    float part[6][NCH];
#pragma unroll
    for (int it = 0; it < 6; ++it) {
      int i = tid + it * 256;
      if (i >= 4 * KVN) i = 4 * KVN - 1;
      int hh = i / KVN, j = i % KVN;
#pragma unroll
      for (int c = 0; c < NCH; ++c) part[it][c] = kvpart[((long)c * NH + hg * 4 + hh) * KVN + j];
    }
#pragma unroll
    for (int it = 0; it < 6; ++it) {
      int i = tid + it * 256;
      if (i < 4 * KVN) {
        int hh = i / KVN, j = i % KVN;
        float sum = 0.f;
#pragma unroll
        for (int c = 0; c < NCH; ++c) sum += part[it][c];
        if (j < DH * DH) kv[hh][j / DH][j % DH] = sum;
        else kv[hh][DH][j - DH * DH] = sum;
      }
    }
  }
  {
    int r = tid >> 2, part = tid & 3;
    const bf16x8* xr = (const bf16x8*)(x + (long)(rb * 64 + r) * D_ + part * 144);
    const bf16x8* gw = (const bf16x8*)(gate_w + part * 144);
    float sum = 0.f;
#pragma unroll
    for (int i = 0; i < 18; ++i) {
      bf16x8 a = xr[i], b = gw[i];
#pragma unroll
      for (int j = 0; j < 8; ++j) sum += bf2f((unsigned short)a[j]) * bf2f((unsigned short)b[j]);
    }
    sum += __shfl_xor(sum, 1);
    sum += __shfl_xor(sum, 2);
    float gg = 1.f / (1.f + __expf(-sum));
    if (part == 0) {
      gs[r] = gg;
      if (hg == 0) gbuf[rb * 64 + r] = gg;
    }
  }
  __syncthreads();
  {
    int r = tid & 63, hh = tid >> 6;
    float pq[DH];
#pragma unroll
    for (int d2 = 0; d2 < DH / 2; ++d2) {
      unsigned w = qw[d2];
      pq[2 * d2] = __uint_as_float(w << 16);
      pq[2 * d2 + 1] = __uint_as_float(w & 0xffff0000u);
    }
    float num[20];
#pragma unroll
    for (int e = 0; e < 20; ++e) num[e] = 0.f;
    float den = 1e-6f;
#pragma unroll
    for (int d = 0; d < DH; ++d) {
      den += pq[d] * kv[hh][DH][d];
#pragma unroll
      for (int e4 = 0; e4 < 5; ++e4) {
        f32x4 kk = *(const f32x4*)&kv[hh][d][4 * e4];
#pragma unroll
        for (int j = 0; j < 4; ++j) num[4 * e4 + j] += pq[d] * kk[j];
      }
    }
    float sc = gs[r] / den;
    unsigned* orow = (unsigned*)(o + (long)(rb * 64 + r) * D_ + (hg * 4 + hh) * DH);
#pragma unroll
    for (int e2 = 0; e2 < DH / 2; ++e2)
      orow[e2] = (unsigned)f2bf(num[2 * e2] * sc) | ((unsigned)f2bf(num[2 * e2 + 1] * sc) << 16);
  }
}

struct G3 {
  static constexpr int A_BYTES = 128 * 128, P_BYTES = 64 * 128, B_BYTES = 3 * P_BYTES;
  static constexpr int STAGE_BYTES = A_BYTES + B_BYTES;
  static constexpr int MT = 4, NT = 3;
  static __device__ __forceinline__ int bswz(int k) { return ((k >> 1) & 1) | (((k >> 3) & 1) << 1); }
  static __device__ __forceinline__ void offsets(unsigned (&aoff)[2], unsigned (&boff)[3], int lda, int wave, int lane) {
#pragma unroll
    for (int i = 0; i < 2; ++i) {
      int off = (wave * 2 + i) * 1024 + lane * 16;
      int row = off >> 7, slot = (off & 127) >> 4;
      int ch = slot ^ ((row >> 1) & 7);
      aoff[i] = (unsigned)(row * lda + ch * 8) * 2u;
    }
#pragma unroll
    for (int i = 0; i < 3; ++i) {
      int off = wave * 1024 + lane * 16;
      int krow = off >> 7, slot = (off & 127) >> 4;
      int ch = slot ^ (bswz(krow) << 1);
      boff[i] = (unsigned)(krow * D_ + i * 64 + ch * 8) * 2u;
    }
  }
  static __device__ __forceinline__ void stage(unsigned lds, const unsigned short* Abase, const unsigned short* Bbase,
                                               const unsigned (&aoff)[2], const unsigned (&boff)[3], int wave) {
#pragma unroll
    for (int i = 0; i < 2; ++i) glds16(aoff[i], Abase, lds + (wave * 2 + i) * 1024);
#pragma unroll
    for (int i = 0; i < 3; ++i) glds16(boff[i], Bbase, lds + A_BYTES + i * P_BYTES + wave * 1024);
  }
  static __device__ __forceinline__ void rd(const char* buf, int s, bf16x8 (&af)[MT], bf16x8 (&bfr)[NT], int wm, int wn, int lane) {
    const int fr = lane & 15, g = lane >> 4, q = (lane & 15) >> 2, p = lane & 3;
#pragma unroll
    for (int c = 0; c < NT; ++c) {
      int u16 = wn * 3 + c;
      const char* pb = buf + A_BYTES + (u16 >> 2) * P_BYTES;
      int u = u16 & 3;
      int k0 = 32 * s + 8 * g + q;
      int k1 = k0 + 4;
      bf16x4 lo = __builtin_amdgcn_ds_read_tr16_b64_v4i16((lds_b4_ptr)(pb + k0 * 128 + 32 * (u ^ bswz(k0)) + 8 * p));
      bf16x4 hi = __builtin_amdgcn_ds_read_tr16_b64_v4i16((lds_b4_ptr)(pb + k1 * 128 + 32 * (u ^ bswz(k1)) + 8 * p));
      bfr[c] = __builtin_shufflevector(lo, hi, 0, 1, 2, 3, 4, 5, 6, 7);
    }
#pragma unroll
    for (int i = 0; i < MT; ++i) {
      int row = wm * 64 + 16 * i + fr;
      int ch = 4 * s + g;
      af[i] = *(const bf16x8*)(buf + row * 128 + 16 * (ch ^ ((row >> 1) & 7)));
    }
    __builtin_amdgcn_sched_barrier(0);
  }
  static __device__ __forceinline__ void mma(f32x4 (&acc)[MT][NT], const bf16x8 (&af)[MT], const bf16x8 (&bfr)[NT]) {
#pragma unroll
    for (int i = 0; i < MT; ++i)
#pragma unroll
      for (int c = 0; c < NT; ++c)
        acc[i][c] = __builtin_amdgcn_mfma_f32_16x16x32_bf16(bfr[c], af[i], acc[i][c], 0, 0, 0);
    __builtin_amdgcn_sched_barrier(0);
  }
};
#define NSPLIT 5

__global__ __launch_bounds__(512, 2) void k3_gemm(const unsigned short* __restrict__ o, const unsigned short* __restrict__ h,
                                                  const unsigned short* __restrict__ wo, const unsigned short* __restrict__ w2,
                                                  const float* __restrict__ gbuf, float* __restrict__ slabs) {
  extern __shared__ __attribute__((aligned(16))) char smem[];
  const int tid = threadIdx.x, wave = __builtin_amdgcn_readfirstlane(tid >> 6), lane = tid & 63;
  const int wm = wave >> 2, wn = wave & 3;
  const int xcd = blockIdx.x & 7, idx = blockIdx.x >> 3;
  const int grp = xcd * 10 + idx / 3;
  const int split = grp % NSPLIT;
  const int n0 = (idx % 3) * 192, m0 = (grp / NSPLIT) * 128;
  const int fr = lane & 15, g = lane >> 4;

  f32x4 acc[G3::MT][G3::NT];
#pragma unroll
  for (int i = 0; i < G3::MT; ++i)
#pragma unroll
    for (int c = 0; c < G3::NT; ++c) acc[i][c] = (f32x4){0.f, 0.f, 0.f, 0.f};

  const int nb = (split == 0) ? 8 : 16;
  const int b0 = (split == 0) ? 0 : 8 + (split - 1) * 16;
  const int nt = (split == 0) ? 17 : 16;

  unsigned hoff[2], woff[3], ooff[2], woff2[3];
  G3::offsets(hoff, woff, DFF, wave, lane);
  G3::offsets(ooff, woff2, D_, wave, lane);
  const unsigned lds0 = (unsigned)(size_t)smem;

  auto do_stage = [&](int t, unsigned lds) {
    if (t < nb) {
      int kt = b0 + t;
      G3::stage(lds, h + (long)m0 * DFF + kt * 64, w2 + (long)kt * 64 * D_ + n0, hoff, woff, wave);
    } else {
      int kt = t - nb;
      G3::stage(lds, o + (long)m0 * D_ + kt * 64, wo + (long)kt * 64 * D_ + n0, ooff, woff, wave);
    }
  };
  auto scale_acc = [&]() {
#pragma unroll
    for (int i = 0; i < G3::MT; ++i) {
      float sc = 1.f - gbuf[m0 + wm * 64 + 16 * i + fr];
#pragma unroll
      for (int c = 0; c < G3::NT; ++c) acc[i][c] *= sc;
    }
  };

  do_stage(0, lds0);
  do_stage(1, lds0 + G3::STAGE_BYTES);
  do_stage(2, lds0 + 2 * G3::STAGE_BYTES);
  bf16x8 fa0[G3::MT], fb0[G3::NT], fa1[G3::MT], fb1[G3::NT];
  WAIT_VM(10);
  __builtin_amdgcn_s_barrier();
  G3::rd(smem, 0, fa0, fb0, wm, wn, lane);
  for (int t = 0; t < nt; ++t) {
    const char* cur = smem + (t & 3) * G3::STAGE_BYTES;
    G3::rd(cur, 1, fa1, fb1, wm, wn, lane);
    G3::mma(acc, fa0, fb0);
    if (t + 1 < nt) {
      WAIT_LGKM0;
      if (t + 2 < nt) { WAIT_VM(5); } else { WAIT_VM(0); }
      __builtin_amdgcn_s_barrier();
      if (t + 3 < nt) do_stage(t + 3, lds0 + ((t + 3) & 3) * G3::STAGE_BYTES);
      G3::rd(smem + ((t + 1) & 3) * G3::STAGE_BYTES, 0, fa0, fb0, wm, wn, lane);
    }
    G3::mma(acc, fa1, fb1);
    if (t == nb - 1) scale_acc();
  }
  float* dst = slabs + (long)split * S_ * D_;
#pragma unroll
  for (int i = 0; i < G3::MT; ++i)
#pragma unroll
    for (int c = 0; c < G3::NT; ++c) {
      int row = m0 + wm * 64 + 16 * i + fr, col = n0 + wn * 48 + 16 * c + 4 * g;
      *(f32x4*)(dst + (long)row * D_ + col) = acc[i][c];
    }
}

__global__ __launch_bounds__(256) void k4_reduce(const float* __restrict__ slabs, unsigned short* __restrict__ out) {
  int i = blockIdx.x * 256 + threadIdx.x;
  const f32x4* s0 = (const f32x4*)slabs;
  f32x4 v = s0[i];
#pragma unroll
  for (int k = 1; k < NSPLIT; ++k) v += s0[i + (long)k * S_ * D_ / 4];
  bf16x4 r;
#pragma unroll
  for (int j = 0; j < 4; ++j) r[j] = (short)f2bf(v[j]);
  ((bf16x4*)out)[i] = r;
}

extern "C" void kernel_launch(void* const* d_in, const int* in_sizes, int n_in, void* d_out, int out_size, void* d_ws,
                              size_t ws_size, hipStream_t stream) {
  const unsigned short* x = (const unsigned short*)d_in[0];
  const unsigned short* wqkv = (const unsigned short*)d_in[1];
  const unsigned short* wo = (const unsigned short*)d_in[2];
  const unsigned short* w1 = (const unsigned short*)d_in[4];
  const unsigned short* w2 = (const unsigned short*)d_in[5];
  const unsigned short* gate_w = (const unsigned short*)d_in[6];
  unsigned short* out = (unsigned short*)d_out;

  char* ws = (char*)d_ws;
  unsigned short* qkv = (unsigned short*)ws;
  unsigned short* h = (unsigned short*)(ws + 8u * 1024 * 1024);
  unsigned short* o = (unsigned short*)(ws + 28u * 1024 * 1024);
  float* gbuf = (float*)(ws + 31u * 1024 * 1024);
  float* kvpart = (float*)(ws + 31u * 1024 * 1024 + 65536);
  float* slabs = (float*)(ws + 32u * 1024 * 1024);

  hipFuncSetAttribute((const void*)k1_gemm, hipFuncAttributeMaxDynamicSharedMemorySize, 2 * G1::STAGE_BYTES);
  hipFuncSetAttribute((const void*)k3_gemm, hipFuncAttributeMaxDynamicSharedMemorySize, 4 * G3::STAGE_BYTES);

  k1_gemm<<<144 + 56, 512, 2 * G1::STAGE_BYTES, stream>>>(x, wqkv, w1, qkv, h);
  k2a_kv<<<128, 256, 0, stream>>>(qkv, kvpart);
  k2b_o<<<256, 256, 0, stream>>>(qkv, kvpart, x, gate_w, o, gbuf);
  k3_gemm<<<240, 512, 4 * G3::STAGE_BYTES, stream>>>(o, h, wo, w2, gbuf, slabs);
  k4_reduce<<<S_ * D_ / 4 / 256, 256, 0, stream>>>(slabs, out);
}
```
